```python
import torch

def _softmax(x, axis=-1):
    return torch.softmax(x, dim=axis)

def _gelu(x):
    return torch.nn.functional.gelu(x, approximate='tanh')

def _silu(x):
    return torch.nn.functional.silu(x)

def _sigmoid(x):
    return torch.sigmoid(x)

def _layernorm(x, axis=-1, eps=1e-05):
    m = x.mean(dim=axis, keepdim=True)
    v = x.var(dim=axis, keepdim=True, unbiased=False)
    return (x - m) / torch.sqrt(v + eps)

def _scatter_add(tgt, idx, src, axis=0):
    return tgt.clone().index_add_(axis, idx, src)

def _segment_sum(src, seg, n):
    out = torch.zeros((n,) + src.shape[1:], dtype=src.dtype)
    return out.index_add_(0, seg, src)

def _pad(x, pw):
    flat = tuple((v for pair in reversed(pw) for v in pair))
    return torch.nn.functional.pad(x, flat)
kernel_name = 'adaLN_cond__sliding_local_dilated__glu_gated_swiglu__prompt'

def setup_inputs():

    _w = lambda a: (lambda t: t.to(torch.bfloat16) if t.is_floating_point() else t)(torch.as_tensor(a))

    def _A_setup():
        B, S, D = (1, 2048, 576)
        return {'x': _w((lambda *s: torch.randn(*s, dtype=torch.bfloat16) * 0.05)(B, S, D)), 'wqkv': _w((lambda *s: torch.randn(*s, dtype=torch.bfloat16) * 0.05)(D, 3 * D)), 'wo': _w((lambda *s: torch.randn(*s, dtype=torch.bfloat16) * 0.05)(D, D))}
    _A_in = _A_setup()

    def _B_setup():
        B, S, D, F = (1, 2048, 576, 4608)
        return {'x': _w((lambda *s: torch.randn(*s, dtype=torch.bfloat16) * 0.05)(B, S, D)), 'w1': _w((lambda *s: torch.randn(*s, dtype=torch.bfloat16) * 0.8)(D, F) * (2.0 / D) ** 0.5), 'w3': _w((lambda *s: torch.randn(*s, dtype=torch.bfloat16) * 0.8)(D, F) * (2.0 / D) ** 0.5), 'w2': _w((lambda *s: torch.randn(*s, dtype=torch.bfloat16) * 0.8)(F, D) * (2.0 / F) ** 0.5)}
    _B_in = _B_setup()
    return {'A_x': _A_in['x'], 'A_wqkv': _A_in['wqkv'], 'A_wo': _A_in['wo'], 'B_x': _B_in['x'], 'B_w1': _B_in['w1'], 'B_w3': _B_in['w3'], 'B_w2': _B_in['w2'], 'c': _w((lambda *s: torch.randn(*s, dtype=torch.bfloat16) * 0.1)(1, 576) * 8.0), 'w_mod': _w((lambda *s: torch.randn(*s, dtype=torch.bfloat16) * 0.1)(576, 6 * 576) * 576 ** (-0.5) * 8.0)}

def reference(A_x, A_wqkv, A_wo, B_x, B_w1, B_w3, B_w2, c, w_mod):

    def _A_ref(x, wqkv, wo):
        B, S, D = x.shape
        H = 32
        Dh = D // H
        W, dil = (8, 2)
        qkv = (lambda x, *d: x.permute(*d))((x @ wqkv).reshape(B, S, 3, H, Dh), 2, 0, 3, 1, 4)
        q, k, v = (qkv[0], qkv[1], qkv[2])
        scale = Dh ** (-0.5)
        pos = torch.arange(S)
        off = torch.arange(W) * dil
        idx = pos[:, None] - off[None, :]
        mask = idx >= 0
        idx_c = torch.where(mask, idx, 0).reshape(-1)
        kg = k[:, :, idx_c, :].reshape(B, H, S, W, Dh)
        vg = v[:, :, idx_c, :].reshape(B, H, S, W, Dh)
        logits = torch.einsum('bhsd,bhswd->bhsw', q, kg) * scale
        logits = torch.where(mask[None, None, :, :], logits, -1000000000.0)
        attn = _softmax(logits, axis=-1)
        o = torch.einsum('bhsw,bhswd->bhsd', attn, vg)
        return (lambda x, *d: x.permute(*d))(o, 0, 2, 1, 3).reshape(B, S, D) @ wo

    def _B_ref(x, w1, w3, w2):
        gate = torch.einsum('bsd,df->bsf', x, w1)
        up = torch.einsum('bsd,df->bsf', x, w3)
        h = _silu(gate) * up
        out = torch.einsum('bsf,fd->bsd', h, w2)
        return out
    x = A_x
    mod = (_silu(c) @ w_mod).reshape(c.shape[0], 6, 1, -1)
    sa, ca, ga, sb, cb, gb = (mod[:, 0].clone(), mod[:, 1].clone(), mod[:, 2].clone(), mod[:, 3].clone(), mod[:, 4].clone(), mod[:, 5].clone())
    h = _layernorm(x, axis=-1) * (1 + ca) + sa
    x = x + ga * _A_ref(h, A_wqkv, A_wo)
    h = _layernorm(x, axis=-1) * (1 + cb) + sb
    x = x + gb * _B_ref(h, B_w1, B_w3, B_w2)
    return x
```

```cpp
namespace k0 {
constexpr int NB_MOD = 162, NB_STAT = 64, NW_PQ = 8 * 4 * 18, NW_PO = 9 * 18, NB_PACK = (NW_PQ + NW_PO + 3) / 4;
constexpr int NB = NB_MOD + NB_STAT + NB_PACK + 1;
}
__global__ __launch_bounds__(256) void k_pre(const u16* __restrict__ x, const u16* __restrict__ wqkv, const u16* __restrict__ wo,
                                             const u16* __restrict__ cvec, const u16* __restrict__ wmod, float* __restrict__ modp,
                                             float* __restrict__ stat1, u16* __restrict__ wqf, u16* __restrict__ wof,
                                             unsigned* __restrict__ flags) {
  using namespace k0;
  __shared__ __attribute__((aligned(16))) char sm[4 * 32 * 160];
  const int tid = threadIdx.x, lane = tid & 63, wave = tid >> 6;
  int b = blockIdx.x;
  if (b < NB_MOD) {
    const int cg = b % 54, ks = b / 54, rg = tid >> 3, cl = tid & 7;
    u32x4 w[6]; float sv[6];
#pragma unroll
    for (int i = 0; i < 6; ++i) {
      const int k = ks * 192 + rg + 32 * i;
      w[i] = *(const u32x4*)(wmod + (size_t)k * 3456 + cg * 64 + cl * 8);
      sv[i] = bf2f(cvec[k]);
    }
    float acc[8] = {};
#pragma unroll
    for (int i = 0; i < 6; ++i) {
      const float s = rbf(silu_f(sv[i]));
#pragma unroll
      for (int j = 0; j < 4; ++j) {
        acc[2 * j] += s * __uint_as_float(w[i][j] << 16);
        acc[2 * j + 1] += s * __uint_as_float(w[i][j] & 0xffff0000u);
      }
    }
#pragma unroll
    for (int j = 0; j < 8; ++j) { acc[j] += __shfl_xor(acc[j], 8); acc[j] += __shfl_xor(acc[j], 16); acc[j] += __shfl_xor(acc[j], 32); }
    float* red = (float*)sm;
    if (lane < 8) {
#pragma unroll
      for (int j = 0; j < 8; ++j) red[wave * 64 + lane * 8 + j] = acc[j];
    }
    __syncthreads();
    if (tid < 64) modp[ks * 3456 + cg * 64 + tid] = red[tid] + red[64 + tid] + red[128 + tid] + red[192 + tid];
    return;
  }
  b -= NB_MOD;
  if (b < NB_STAT) {
    const int row = (b * 4 + wave) * 8 + (lane >> 3), cl = lane & 7;
    u32x4 v[9];
#pragma unroll
    for (int i = 0; i < 9; ++i) v[i] = *(const u32x4*)(x + (size_t)row * D_ + (cl + 8 * i) * 8);
    float s = 0.f;
#pragma unroll
    for (int i = 0; i < 9; ++i)
#pragma unroll
      for (int j = 0; j < 4; ++j) s += __uint_as_float(v[i][j] << 16) + __uint_as_float(v[i][j] & 0xffff0000u);
    s += __shfl_xor(s, 1); s += __shfl_xor(s, 2); s += __shfl_xor(s, 4);
    const float mean = s * (1.f / D_);
    float q = 0.f;
#pragma unroll
    for (int i = 0; i < 9; ++i)
#pragma unroll
      for (int j = 0; j < 4; ++j) {
        const float a = __uint_as_float(v[i][j] << 16) - mean, c = __uint_as_float(v[i][j] & 0xffff0000u) - mean;
        q += a * a + c * c;
      }
    q += __shfl_xor(q, 1); q += __shfl_xor(q, 2); q += __shfl_xor(q, 4);
    const float rstd = __builtin_amdgcn_rsqf(q * (1.f / D_) + 1e-5f);
    if (cl == 0) { stat1[2 * row] = rstd; stat1[2 * row + 1] = -mean * rstd; }
    return;
  }
  b -= NB_STAT;
  if (b < NB_PACK) {
    const int t = b * 4 + wave;
    if (t >= NW_PQ + NW_PO) return;
    char* img = sm + wave * (32 * 160);
    const bool isq = t < NW_PQ;
    int kap, grp, hg = 0;
    if (isq) { kap = t % 18; grp = (t / 18) & 3; hg = t / 72; } else { kap = (t - NW_PQ) % 18; grp = (t - NW_PQ) / 18; }
    u32x4 v[4];
#pragma unroll
    for (int i = 0; i < 4; ++i) {
      const int row = i * 8 + (lane >> 3), ch = lane & 7;
      const u16* src;
      if (isq) {
        int c8 = grp * 8 + ch; if (c8 > 26) c8 = 26;
        const int seg = c8 / 9, cc = c8 % 9;
        src = wqkv + (size_t)(kap * 32 + row) * 1728 + seg * 576 + hg * 72 + cc * 8;
      } else {
        src = wo + (size_t)(kap * 32 + row) * D_ + grp * 64 + ch * 8;
      }
      v[i] = *(const u32x4*)src;
    }
#pragma unroll
    for (int i = 0; i < 4; ++i) *(u32x4*)(img + (i * 8 + (lane >> 3)) * 160 + (lane & 7) * 16) = v[i];
    asm volatile("s_waitcnt lgkmcnt(0)" ::: "memory");
    const unsigned a0 = (unsigned)(uintptr_t)(LAS char*)img + (8 * (lane >> 4) + ((lane & 15) >> 2)) * 160 + 8 * (lane & 3);
#pragma unroll
    for (int f = 0; f < 4; ++f) {
      const bf16x8 fr = tr_frag(a0 + f * 32, a0 + f * 32 + 4 * 160);
      const int nf = grp * 4 + f;
      if (isq) { if (nf < 14) *(bf16x8*)(wqf + ((size_t)((hg * 14 + nf) * 18 + kap) * 64 + lane) * 8) = fr; }
      else *(bf16x8*)(wof + ((size_t)(nf * 18 + kap) * 64 + lane) * 8) = fr;
    }
    return;
  }
  for (int i = tid; i < FLAG_WORDS / 4; i += 256) ((u32x4*)flags)[i] = (u32x4){0u, 0u, 0u, 0u};
}

namespace p1 {
constexpr int XS = 0;
constexpr int MODS = 92160;
constexpr int QP = 456;
constexpr int OS = 40960;
constexpr int OP = 160;
constexpr int LDS_BYTES = MODS + 4608;
}
template <int NN, int M0>
DI void p1_gemm(char* smem, const bf16x8 (&wf)[2][18], f32x4 (&acc)[2][5], int lane) {
  const char* xs = smem + p1::XS + lane * 16;
  static_for<18>([&](auto kc) LAMBDA_INLINE {
    constexpr int k = decltype(kc)::value;
    bf16x8 bf[5];
#pragma unroll
    for (int m = M0; m < 5; ++m) bf[m] = *(const bf16x8*)(xs + (k * 5 + m) * 1024);
#pragma unroll
    for (int n = 0; n < NN; ++n)
#pragma unroll
      for (int m = M0; m < 5; ++m) acc[n][m] = __builtin_amdgcn_mfma_f32_16x16x32_bf16(wf[n][k], bf[m], acc[n][m], 0, 0, 0);
  });
}

template <int AUX>
DI void phase1(char* smem, const u16* __restrict__ x, const float* __restrict__ modp, const float* __restrict__ stat1,
               const u16* __restrict__ wqf, u16* __restrict__ of, int rt, int hg, int tid, int lane, int wave) {
  using namespace p1;
  const int g = lane >> 4, m16 = lane & 15;
  const int r0 = rt * 64 - 16;
  f32x4 mp[3];
  if (tid < 288) {
#pragma unroll
    for (int i = 0; i < 3; ++i) mp[i] = *(const f32x4*)(modp + i * 3456 + tid * 4);
  }
  u32x4 xv[12];
  float rs[5], nm[5];
#pragma unroll
  for (int m = 0; m < 5; ++m) {
    int row = r0 + m * 16 + m16; if (row < 0) row = 0;
    const u16* xr = x + (size_t)row * D_ + 8 * g;
    xv[2 * m] = *(const u32x4*)(xr + 64 * wave);
    xv[2 * m + 1] = *(const u32x4*)(xr + 64 * wave + 32);
    rs[m] = stat1[2 * row]; nm[m] = stat1[2 * row + 1];
  }
  const int kx = 16 + (wave & 1), mx = wave >> 1;
  float rsx, nmx;
  {
    int row = r0 + mx * 16 + m16; if (row < 0) row = 0;
    xv[10] = *(const u32x4*)(x + (size_t)row * D_ + 8 * g + 32 * kx);
    rsx = stat1[2 * row]; nmx = stat1[2 * row + 1];
    xv[11] = *(const u32x4*)(x + (size_t)(r0 + 64 + m16) * D_ + 8 * g + 32 * kx);
  }
  const int nf0 = wave < 4 ? 4 + 2 * wave : wave < 6 ? 2 * (wave - 4) : 6 + wave;
  bf16x8 wf[2][18];
  {
    const bf16x8* wp = (const bf16x8*)wqf + (size_t)(hg * 14 + nf0) * 18 * 64 + lane;
#pragma unroll
    for (int k = 0; k < 18; ++k) {
      wf[0][k] = wp[k * 64];
      wf[1][k] = wp[(wave < 6 ? 18 + k : k) * 64];
    }
  }
  float* mods = (float*)(smem + MODS);
  if (tid < 288) {
    f32x4 v = mp[0] + mp[1] + mp[2];
#pragma unroll
    for (int j = 0; j < 4; ++j) { v[j] = rbf(v[j]); if (tid >= 144) v[j] = rbf(1.f + v[j]); }
    *(f32x4*)(mods + tid * 4) = v;
  }
  __syncthreads();
  auto norm8 = [&](u32x4 v, float r, float n, const f32x4 (&sa)[2], const f32x4 (&aa)[2]) LAMBDA_INLINE {
    u32x4 o;
#pragma unroll
    for (int j = 0; j < 4; ++j) {
      const float lo = __uint_as_float(v[j] << 16) * r + n, hi = __uint_as_float(v[j] & 0xffff0000u) * r + n;
      o[j] = pack_bf16(lo * aa[j >> 1][(2 * j) & 3] + sa[j >> 1][(2 * j) & 3], hi * aa[j >> 1][(2 * j + 1) & 3] + sa[j >> 1][(2 * j + 1) & 3]);
    }
    return o;
  };
#pragma unroll
  for (int kk = 0; kk < 3; ++kk) {
    const int kap = kk < 2 ? 2 * wave + kk : kx;
    f32x4 sa[2], aa[2];
    sa[0] = *(const f32x4*)(mods + kap * 32 + 8 * g); sa[1] = *(const f32x4*)(mods + kap * 32 + 8 * g + 4);
    aa[0] = *(const f32x4*)(mods + 576 + kap * 32 + 8 * g); aa[1] = *(const f32x4*)(mods + 576 + kap * 32 + 8 * g + 4);
    if (kk < 2) {
#pragma unroll
      for (int m = 0; m < 5; ++m) *(u32x4*)(smem + XS + (kap * 5 + m) * 1024 + lane * 16) = norm8(xv[2 * m + kk], rs[m], nm[m], sa, aa);
    } else {
      *(u32x4*)(smem + XS + (kap * 5 + mx) * 1024 + lane * 16) = norm8(xv[10], rsx, nmx, sa, aa);
      if (wave < 2) *(u32x4*)(smem + XS + (kap * 5 + 4) * 1024 + lane * 16) = norm8(xv[11], rs[4], nm[4], sa, aa);
    }
  }
  __syncthreads();
  f32x4 acc[2][5] = {};
  if (wave < 4) p1_gemm<2, 0>(smem, wf, acc, lane);
  else if (wave < 6) p1_gemm<2, 1>(smem, wf, acc, lane);
  else p1_gemm<1, 0>(smem, wf, acc, lane);
  __syncthreads();
#pragma unroll
  for (int n = 0; n < 2; ++n)
#pragma unroll
    for (int m = 0; m < 5; ++m) {
      if (n == 1 && wave >= 6) continue;
      u32x2 v = {pack_bf16(acc[n][m][0], acc[n][m][1]), pack_bf16(acc[n][m][2], acc[n][m][3])};
      *(u32x2*)(smem + (m * 16 + m16) * QP + ((nf0 + n) * 16 + 4 * g) * 2) = v;
    }
  __syncthreads();
  {
    typedef __bf16 bf2 __attribute__((ext_vector_type(2)));
    const int rl = (wave >> 1) * 16 + m16, hd = (wave & 1) * 2 + (g & 1), hw = lane >> 5;
    const int grow = rt * 64 + rl;
    const unsigned* qrow = (const unsigned*)(smem + (rl + 16) * QP) + hd * 9;
    unsigned qv[9];
#pragma unroll
    for (int d = 0; d < 9; ++d) qv[d] = qrow[d];
    float lg[4];
#pragma unroll
    for (int w = 0; w < 4; ++w) {
      const int ww = hw * 4 + w;
      const unsigned* kr = (const unsigned*)(smem + (rl + 16 - 2 * ww) * QP) + 36 + hd * 9;
      float a = 0.f;
#pragma unroll
      for (int d = 0; d < 9; ++d) a = __builtin_amdgcn_fdot2_f32_bf16(__builtin_bit_cast(bf2, qv[d]), __builtin_bit_cast(bf2, kr[d]), a, false);
      a *= 0.23570226039551584f;
      lg[w] = (grow - 2 * ww >= 0) ? a : -1e9f;
    }
    float mxv = fmaxf(fmaxf(lg[0], lg[1]), fmaxf(lg[2], lg[3]));
    mxv = fmaxf(mxv, __shfl_xor(mxv, 32));
    float pw[8], sum = 0.f;
#pragma unroll
    for (int w = 0; w < 4; ++w) { lg[w] = __builtin_amdgcn_exp2f((lg[w] - mxv) * 1.44269504089f); sum += lg[w]; }
    sum += __shfl_xor(sum, 32);
    const float inv = __builtin_amdgcn_rcpf(sum);
#pragma unroll
    for (int w = 0; w < 4; ++w) {
      const float mine = lg[w] * inv, other = __shfl_xor(mine, 32);
      pw[w] = hw ? other : mine; pw[4 + w] = hw ? mine : other;
    }
    float o[10] = {};
#pragma unroll
    for (int w = 0; w < 8; ++w) {
      const unsigned* vr = (const unsigned*)(smem + (rl + 16 - 2 * w) * QP) + 72 + hd * 9 + hw * 5;
#pragma unroll
      for (int d = 0; d < 5; ++d) {
        const unsigned v = vr[d];
        o[2 * d] += pw[w] * __uint_as_float(v << 16);
        o[2 * d + 1] += pw[w] * __uint_as_float(v & 0xffff0000u);
      }
    }
    unsigned* orow = (unsigned*)(smem + OS + rl * OP) + hd * 9 + hw * 5;
#pragma unroll
    for (int d = 0; d < 5; ++d) if (d < 4 || hw == 0) orow[d] = pack_bf16(o[2 * d], o[2 * d + 1]);
  }
  __syncthreads();
  const __amdgpu_buffer_rsrc_t ors = __builtin_amdgcn_make_buffer_rsrc((void*)of, 0, S_ * D_ * 2, 0x00020000);
#pragma unroll
  for (int it = 0; it < 2; ++it) {
    const int pc = tid + it * 512;
    if (pc < 576) {
      const int row = pc & 63, c = pc >> 6;
      const u32x4 v = *(const u32x4*)(smem + OS + row * OP + c * 16);
      const int c8 = hg * 9 + c, mf = rt * 4 + (row >> 4);
      st16<AUX>(ors, (unsigned)(((mf * 18 + (c8 >> 2)) * 64 + (c8 & 3) * 16 + (row & 15)) * 16), 0, v);
    }
  }
}

__global__ __launch_bounds__(512, 2) void k_p1(const u16* __restrict__ x, const float* __restrict__ modp, const float* __restrict__ stat1,
                                               const u16* __restrict__ wqf, u16* __restrict__ of) {
  extern __shared__ __attribute__((aligned(16))) char smem[];
  const int tid = threadIdx.x, lane = tid & 63, wave = __builtin_amdgcn_readfirstlane(tid >> 6);
  phase1<0>(smem, x, modp, stat1, wqf, of, blockIdx.x >> 3, blockIdx.x & 7, tid, lane, wave);
}

namespace p2 {
constexpr int OSM = 0;
constexpr int RED = 36864;
constexpr int LDS_BYTES = RED + 9 * 32 * 8 + 32 * 8;
}
__global__ __launch_bounds__(512, 2) void k_p2a(const u16* __restrict__ x, const float* __restrict__ modp, const u16* __restrict__ wof,
                                                const u16* __restrict__ of, float* __restrict__ x1f, float* __restrict__ st2) {
  using namespace p2;
  extern __shared__ __attribute__((aligned(16))) char smem[];
  const int tid = threadIdx.x, lane = tid & 63, wave = __builtin_amdgcn_readfirstlane(tid >> 6);
  const int b = blockIdx.x, xcd = b & 7, idx = b >> 3, rt = (idx >> 2) * 8 + xcd, nt = idx & 3;
  const int g = lane >> 4, m16 = lane & 15;
  bf16x8 wf[2][18];
  {
    const bf16x8* wp = (const bf16x8*)wof + (size_t)(nt * 9 + wave) * 18 * 64 + lane;
    const bf16x8* wp8 = (const bf16x8*)wof + (size_t)(nt * 9 + 8) * 18 * 64 + lane;
#pragma unroll
    for (int k = 0; k < 18; ++k) { wf[0][k] = wp[k * 64]; if (wave < 2) wf[1][k] = wp8[k * 64]; }
  }
  u32x4 ov[5];
#pragma unroll
  for (int i = 0; i < 5; ++i) {
    const int f = wave + 8 * i;
    if (f < 36) ov[i] = *((const u32x4*)of + ((size_t)rt * 36 + f) * 64 + lane);
  }
  u32x2 xr[3]; f32x4 ga[2];
#pragma unroll
  for (int t = 0; t < 3; ++t) {
    const int nf = t < 2 ? wave : 8, mi = t < 2 ? t : (wave & 1);
    xr[t] = *(const u32x2*)(x + (size_t)(rt * 32 + mi * 16 + m16) * D_ + nt * 144 + nf * 16 + 4 * g);
  }
#pragma unroll
  for (int t = 0; t < 2; ++t) {
    const float* mp = modp + 2 * D_ + nt * 144 + (t ? 8 : wave) * 16 + 4 * g;
    ga[t] = *(const f32x4*)mp + *(const f32x4*)(mp + 3456) + *(const f32x4*)(mp + 2 * 3456);
  }
#pragma unroll
  for (int i = 0; i < 5; ++i) {
    const int f = wave + 8 * i;
    if (f < 36) *(u32x4*)(smem + OSM + f * 1024 + lane * 16) = ov[i];
  }
  __syncthreads();
  f32x4 acc[3] = {};
#pragma unroll
  for (int k = 0; k < 18; ++k) {
    const bf16x8 b0 = *(const bf16x8*)(smem + OSM + k * 1024 + lane * 16), b1 = *(const bf16x8*)(smem + OSM + (18 + k) * 1024 + lane * 16);
    acc[0] = __builtin_amdgcn_mfma_f32_16x16x32_bf16(wf[0][k], b0, acc[0], 0, 0, 0);
    acc[1] = __builtin_amdgcn_mfma_f32_16x16x32_bf16(wf[0][k], b1, acc[1], 0, 0, 0);
    if (wave < 2) acc[2] = __builtin_amdgcn_mfma_f32_16x16x32_bf16(wf[1][k], wave ? b1 : b0, acc[2], 0, 0, 0);
  }
  float2* red = (float2*)(smem + RED);
#pragma unroll
  for (int t = 0; t < 3; ++t) {
    if (t == 2 && wave >= 2) continue;
    const int nf = t < 2 ? wave : 8, mi = t < 2 ? t : (wave & 1);
    const f32x4 gv = ga[t >> 1];
    f32x4 v;
    v[0] = rbf(__uint_as_float(xr[t][0] << 16) + rbf(gv[0]) * acc[t][0]);
    v[1] = rbf(__uint_as_float(xr[t][0] & 0xffff0000u) + rbf(gv[1]) * acc[t][1]);
    v[2] = rbf(__uint_as_float(xr[t][1] << 16) + rbf(gv[2]) * acc[t][2]);
    v[3] = rbf(__uint_as_float(xr[t][1] & 0xffff0000u) + rbf(gv[3]) * acc[t][3]);
    ((f32x4*)x1f)[((((size_t)rt * 2 + mi) * 4 + nt) * 9 + nf) * 64 + lane] = v;
    float s = v[0] + v[1] + v[2] + v[3];
    s += __shfl_xor(s, 16); s += __shfl_xor(s, 32);
    const float mu = s * (1.f / 16.f);
    float q = (v[0] - mu) * (v[0] - mu) + (v[1] - mu) * (v[1] - mu) + (v[2] - mu) * (v[2] - mu) + (v[3] - mu) * (v[3] - mu);
    q += __shfl_xor(q, 16); q += __shfl_xor(q, 32);
    if (g == 0) red[nf * 32 + mi * 16 + m16] = make_float2(mu, q);
  }
  __syncthreads();
  if (tid < 32) {
    float mu = 0.f;
#pragma unroll
    for (int nf = 0; nf < 9; ++nf) mu += red[nf * 32 + tid].x;
    mu *= (1.f / 9.f);
    float q = 0.f;
#pragma unroll
    for (int nf = 0; nf < 9; ++nf) { const float2 e = red[nf * 32 + tid]; q += e.y + 16.f * (e.x - mu) * (e.x - mu); }
    ((float2*)st2)[(rt * 32 + tid) * 4 + nt] = make_float2(mu, q);
  }
}
__global__ __launch_bounds__(256) void k_p2b(const float* __restrict__ x1f, const float* __restrict__ st2, const float* __restrict__ modp,
                                             u16* __restrict__ h2f) {
  const int gid = blockIdx.x * 256 + threadIdx.x;
  const int lane = gid & 63, fr = gid >> 6, nf = fr % 9, t2 = fr / 9, nt = t2 & 3, mf = t2 >> 2;
  const int m = mf * 16 + (lane & 15), n = nt * 144 + nf * 16 + 4 * (lane >> 4);
  const f32x4 s0 = ((const f32x4*)st2)[m * 2], s1 = ((const f32x4*)st2)[m * 2 + 1];
  const float mu = 0.25f * (s0[0] + s0[2] + s1[0] + s1[2]);
  const float q = s0[1] + s0[3] + s1[1] + s1[3] + 144.f * ((s0[0] - mu) * (s0[0] - mu) + (s0[2] - mu) * (s0[2] - mu) + (s1[0] - mu) * (s1[0] - mu) + (s1[2] - mu) * (s1[2] - mu));
  const float rstd = __builtin_amdgcn_rsqf(q * (1.f / D_) + 1e-5f);
  const f32x4 v = ((const f32x4*)x1f)[gid];
  const float* ps = modp + 3 * D_ + n; const float* pc = modp + 4 * D_ + n;
  const f32x4 sb = *(const f32x4*)ps + *(const f32x4*)(ps + 3456) + *(const f32x4*)(ps + 6912);
  const f32x4 cb = *(const f32x4*)pc + *(const f32x4*)(pc + 3456) + *(const f32x4*)(pc + 6912);
  float h[4];
#pragma unroll
  for (int j = 0; j < 4; ++j) h[j] = (v[j] - mu) * rstd * rbf(1.f + rbf(cb[j])) + rbf(sb[j]);
  const int nfg = nt * 9 + nf;
  u32x2 o = {pack_bf16(h[0], h[1]), pack_bf16(h[2], h[3])};
  *(u32x2*)(h2f + (((size_t)mf * 18 + (nfg >> 1)) * 64 + lane) * 8 + (nfg & 1) * 4) = o;
}

extern "C" void kernel_launch(void* const* d_in, const int* in_sizes, int n_in, void* d_out, int out_size, void* d_ws,
                              size_t ws_size, hipStream_t stream) {
  const u16* x = (const u16*)d_in[0];
  const u16* wqkv = (const u16*)d_in[1];
  const u16* wo = (const u16*)d_in[2];
  const u16* w1 = (const u16*)d_in[4];
  const u16* w3 = (const u16*)d_in[5];
  const u16* w2 = (const u16*)d_in[6];
  const u16* c = (const u16*)d_in[7];
  const u16* wmod = (const u16*)d_in[8];
  char* ws = (char*)d_ws;
  float* modp = (float*)(ws + WS_MODP);
  float* stat1 = (float*)(ws + WS_STAT1);
  u16* of = (u16*)(ws + WS_OF);
  float* x1f = (float*)(ws + WS_X1);
  u16* h2f = (u16*)(ws + WS_H2F);
  u16* Hf = (u16*)(ws + WS_HF);
  u16* part = (u16*)(ws + WS_PART);
  u16* wqf = (u16*)(ws + WS_WQF);
  u16* wof = (u16*)(ws + WS_WOF);
  float* st2 = (float*)(ws + WS_ST2);
  unsigned* flags = (unsigned*)(ws + WS_FLAGS);
  (void)hipFuncSetAttribute((const void*)k_p1, hipFuncAttributeMaxDynamicSharedMemorySize, p1::LDS_BYTES);
  (void)hipFuncSetAttribute((const void*)k_mlp<1, LEAD0_>, hipFuncAttributeMaxDynamicSharedMemorySize, mlp::LDS_BYTES);
  (void)hipFuncSetAttribute((const void*)k_mlp<2, LEAD0_>, hipFuncAttributeMaxDynamicSharedMemorySize, mlp::LDS_BYTES);
  k_pre<<<k0::NB, 256, 0, stream>>>(x, wqkv, wo, c, wmod, modp, stat1, wqf, wof, flags);
  k_p1<<<256, 512, p1::LDS_BYTES, stream>>>(x, modp, stat1, wqf, of);
  k_p2a<<<256, 512, p2::LDS_BYTES, stream>>>(x, modp, wof, of, x1f, st2);
  k_p2b<<<128 * 4 * 9 * 64 / 256, 256, 0, stream>>>(x1f, st2, modp, h2f);
  k_mlp<1, LEAD0_><<<256, 512, mlp::LDS_BYTES, stream>>>(h2f, w1, w3, w2, Hf, part);
  k_mlp<2, LEAD0_><<<256, 512, mlp::LDS_BYTES, stream>>>(h2f, w1, w3, w2, Hf, part);
  k_final<<<64 * 4 * 9 * 64 / 256, 256, 0, stream>>>(part, x1f, modp, (u16*)d_out);
}
```

```cpp
#include <hip/hip_runtime.h>
#include <hip/hip_bf16.h>
#include <stdint.h>
#include <utility>

typedef unsigned short u16;
typedef unsigned long long u64;
typedef short bf16x8 __attribute__((ext_vector_type(8)));
typedef short s16x4 __attribute__((ext_vector_type(4)));
typedef float f32x4 __attribute__((ext_vector_type(4)));
typedef unsigned u32x4 __attribute__((ext_vector_type(4)));
typedef unsigned u32x2 __attribute__((ext_vector_type(2)));
#define LAS __attribute__((address_space(3)))
#define GAS __attribute__((address_space(1)))
#define DI __device__ __forceinline__
#define LAMBDA_INLINE __attribute__((always_inline))
#define IC(x) std::integral_constant<int, (x)>{}
#define RLX_AGENT __ATOMIC_RELAXED, __HIP_MEMORY_SCOPE_AGENT

constexpr int S_ = 2048, D_ = 576, F_ = 4608;

#define STAMP_AT(i) do {} while (0)

template <int... Is, class Fn> DI void static_for_impl(std::integer_sequence<int, Is...>, Fn&& f) { (f(std::integral_constant<int, Is>{}), ...); }
template <int N, class Fn> DI void static_for(Fn&& f) { static_for_impl(std::make_integer_sequence<int, N>{}, f); }

DI float bf2f(u16 v) { return __uint_as_float(((unsigned)v) << 16); }
DI float bflo(unsigned v) { return __uint_as_float(v << 16); }
DI float bfhi(unsigned v) { return __uint_as_float(v & 0xffff0000u); }
DI unsigned pack_bf16(float lo, float hi) {
  typedef __bf16 bf2 __attribute__((ext_vector_type(2)));
  typedef float f2 __attribute__((ext_vector_type(2)));
  f2 v = {lo, hi};
  bf2 r = __builtin_convertvector(v, bf2);
  return __builtin_bit_cast(unsigned, r);
}
DI float rbf(float f) { return __uint_as_float(pack_bf16(f, 0.f) << 16); }
DI float silu_f(float g) { return g * __builtin_amdgcn_rcpf(1.f + __builtin_amdgcn_exp2f(g * -1.44269504089f)); }

template <int N> DI void wait_vm() { asm volatile("s_waitcnt vmcnt(%0)" ::"i"(N) : "memory"); }
DI void barrier_raw() { asm volatile("" ::: "memory"); __builtin_amdgcn_s_barrier(); asm volatile("" ::: "memory"); }
DI void lds_barrier() { asm volatile("s_waitcnt lgkmcnt(0)" ::: "memory"); __builtin_amdgcn_s_barrier(); asm volatile("" ::: "memory"); }
DI void sched_fence() { __builtin_amdgcn_sched_barrier(0); }
DI int lane_now() {
  int l = __builtin_amdgcn_mbcnt_hi(~0u, __builtin_amdgcn_mbcnt_lo(~0u, 0u));
  asm volatile("" : "+v"(l));
  return l;
}

DI void glds16(unsigned voff, const void* sbase, unsigned lds_dst) {
  unsigned keep;
  asm volatile("s_mov_b32 %0, m0\n\ts_mov_b32 m0, %3\n\ts_nop 0\n\tglobal_load_lds_dwordx4 %1, %2\n\ts_mov_b32 m0, %0"
               : "=&s"(keep) : "v"(voff), "s"(sbase), "s"(lds_dst) : "memory");
}
DI void glds16v(const void* vaddr, unsigned lds_dst) {
  unsigned keep;
  asm volatile("s_mov_b32 %0, m0\n\ts_mov_b32 m0, %2\n\ts_nop 0\n\tglobal_load_lds_dwordx4 %1, off\n\ts_mov_b32 m0, %0"
               : "=&s"(keep) : "v"(vaddr), "s"(lds_dst) : "memory");
}
template <int IMM> DI void gload16_sc1(bf16x8& dst, unsigned voff, const void* sbase) {
  asm volatile("global_load_dwordx4 %0, %1, %2 offset:%3 sc1" : "=v"(dst) : "v"(voff), "s"(sbase), "i"(IMM) : "memory");
}
DI void gload8_sc1(u32x2& dst, unsigned voff, const void* sbase) {
  asm volatile("global_load_dwordx2 %0, %1, %2 sc1" : "=v"(dst) : "v"(voff), "s"(sbase) : "memory");
}
DI void gload4_sc1(unsigned& dst, unsigned voff, const void* sbase) {
  asm volatile("global_load_dword %0, %1, %2 sc1" : "=v"(dst) : "v"(voff), "s"(sbase) : "memory");
}
DI s16x4 tr_read(unsigned addr) { return __builtin_amdgcn_ds_read_tr16_b64_v4i16((LAS s16x4*)(uintptr_t)addr); }
DI bf16x8 tr_frag(unsigned a0, unsigned a1) {
  s16x4 lo = tr_read(a0), hi = tr_read(a1);
  return __builtin_shufflevector(lo, hi, 0, 1, 2, 3, 4, 5, 6, 7);
}
typedef __amdgpu_buffer_rsrc_t rsrc_t;
DI rsrc_t mk_rsrc(const void* p, unsigned bytes) { return __builtin_amdgcn_make_buffer_rsrc((void*)p, 0, bytes, 0x00020000); }
DI void st16_sc1(rsrc_t r, unsigned voff, unsigned soff, u32x4 v) {
  __builtin_amdgcn_raw_buffer_store_b128(v, r, voff, soff, 16);
  asm volatile("s_nop 1" ::"v"(v) : "memory");
}
DI void st8_sc1(rsrc_t r, unsigned voff, unsigned soff, u32x2 v) { __builtin_amdgcn_raw_buffer_store_b64(v, r, voff, soff, 16); }
DI u32x4 ld16_sc1(rsrc_t r, unsigned voff, unsigned soff) { return __builtin_amdgcn_raw_buffer_load_b128(r, voff, soff, 16); }
DI u32x2 ld8_sc1(rsrc_t r, unsigned voff, unsigned soff) { return __builtin_amdgcn_raw_buffer_load_b64(r, voff, soff, 16); }

constexpr unsigned SPIN_CAP = 1u << 15;
DI void wait_flags(const unsigned* f, int n, unsigned need, int lane) {
  const GAS unsigned* p = (const GAS unsigned*)f + (lane < n ? lane : 0);
  for (unsigned spin = 0; spin < SPIN_CAP; ++spin) {
    const unsigned v = __hip_atomic_load(p, RLX_AGENT);
    if (__all(v >= need)) break;
    __builtin_amdgcn_s_sleep(1);
  }
  asm volatile("" ::: "memory");
}
DI void set_flag(unsigned* f, unsigned v) { __hip_atomic_store((GAS unsigned*)f, v, RLX_AGENT); }

constexpr size_t WS_MODP = 0;
constexpr size_t WS_STAT1 = 65536;
constexpr size_t WS_FLAGS = 131072;
constexpr size_t WS_OF = 1u << 20;
constexpr size_t WS_H2F = 4u << 20;
constexpr size_t WS_WQF = 8u << 20;
constexpr size_t WS_WOF = 12u << 20;
constexpr size_t WS_HF = 16u << 20;
constexpr size_t WS_PART = 40u << 20;
constexpr int FL_O = 0;
constexpr int FL_H2 = 512;
constexpr int FL_H = 1536;
constexpr int FL_P = FL_H + 8192;
constexpr int FL_ST = FL_P + 4096;
constexpr int FLAG_WORDS = FL_ST + 16384;

namespace k0 {
constexpr int NB_MOD = 162, NB_STAT = 64, NW_PQ = 8 * 4 * 18, NW_PO = 9 * 18, NB_PACK = (NW_PQ + NW_PO + 3) / 4, NB_FLAG = 8;
constexpr int NB = NB_MOD + NB_STAT + NB_PACK + NB_FLAG;
}
__global__ __launch_bounds__(256) void k_pre(const u16* __restrict__ x, const u16* __restrict__ wqkv, const u16* __restrict__ wo,
                                             const u16* __restrict__ cvec, const u16* __restrict__ wmod, char* __restrict__ ws) {
  using namespace k0;
  __shared__ __attribute__((aligned(16))) char sm[4 * 32 * 160];
  const int tid = threadIdx.x, lane = tid & 63, wave = tid >> 6;
  int b = blockIdx.x;
  if (b < NB_MOD) {
    float* modp = (float*)(ws + WS_MODP);
    const int cg = b % 54, ks = b / 54, rg = tid >> 3, cl = tid & 7;
    u32x4 w[6]; float sv[6];
#pragma unroll
    for (int i = 0; i < 6; ++i) {
      const int k = ks * 192 + rg + 32 * i;
      w[i] = *(const u32x4*)(wmod + (size_t)k * 3456 + cg * 64 + cl * 8);
      sv[i] = bf2f(cvec[k]);
    }
    float acc[8] = {};
#pragma unroll
    for (int i = 0; i < 6; ++i) {
      const float s = rbf(silu_f(sv[i]));
#pragma unroll
      for (int j = 0; j < 4; ++j) { acc[2 * j] += s * bflo(w[i][j]); acc[2 * j + 1] += s * bfhi(w[i][j]); }
    }
#pragma unroll
    for (int j = 0; j < 8; ++j) { acc[j] += __shfl_xor(acc[j], 8); acc[j] += __shfl_xor(acc[j], 16); acc[j] += __shfl_xor(acc[j], 32); }
    float* red = (float*)sm;
    if (lane < 8) {
#pragma unroll
      for (int j = 0; j < 8; ++j) red[wave * 64 + lane * 8 + j] = acc[j];
    }
    __syncthreads();
    if (tid < 64) modp[ks * 3456 + cg * 64 + tid] = red[tid] + red[64 + tid] + red[128 + tid] + red[192 + tid];
    return;
  }
  b -= NB_MOD;
  if (b < NB_STAT) {
    float* stat1 = (float*)(ws + WS_STAT1);
    const int row = (b * 4 + wave) * 8 + (lane >> 3), cl = lane & 7;
    u32x4 v[9];
#pragma unroll
    for (int i = 0; i < 9; ++i) v[i] = *(const u32x4*)(x + (size_t)row * D_ + (cl + 8 * i) * 8);
    float s = 0.f;
#pragma unroll
    for (int i = 0; i < 9; ++i)
#pragma unroll
      for (int j = 0; j < 4; ++j) s += bflo(v[i][j]) + bfhi(v[i][j]);
    s += __shfl_xor(s, 1); s += __shfl_xor(s, 2); s += __shfl_xor(s, 4);
    const float mean = s * (1.f / D_);
    float q = 0.f;
#pragma unroll
    for (int i = 0; i < 9; ++i)
#pragma unroll
      for (int j = 0; j < 4; ++j) { const float a = bflo(v[i][j]) - mean, c = bfhi(v[i][j]) - mean; q += a * a + c * c; }
    q += __shfl_xor(q, 1); q += __shfl_xor(q, 2); q += __shfl_xor(q, 4);
    const float rstd = __builtin_amdgcn_rsqf(q * (1.f / D_) + 1e-5f);
    if (cl == 0) { stat1[2 * row] = rstd; stat1[2 * row + 1] = -mean * rstd; }
    return;
  }
  b -= NB_STAT;
  if (b < NB_PACK) {
    u16* wqf = (u16*)(ws + WS_WQF);
    u16* wof = (u16*)(ws + WS_WOF);
    const int t = b * 4 + wave;
    if (t >= NW_PQ + NW_PO) return;
    char* img = sm + wave * (32 * 160);
    const bool isq = t < NW_PQ;
    int kap, grp, hg = 0;
    if (isq) { kap = t % 18; grp = (t / 18) & 3; hg = t / 72; } else { kap = (t - NW_PQ) % 18; grp = (t - NW_PQ) / 18; }
    u32x4 v[4];
#pragma unroll
    for (int i = 0; i < 4; ++i) {
      const int row = i * 8 + (lane >> 3), ch = lane & 7;
      const u16* src;
      if (isq) {
        int c8 = grp * 8 + ch; if (c8 > 26) c8 = 26;
        const int seg = c8 / 9, cc = c8 % 9;
        src = wqkv + (size_t)(kap * 32 + row) * 1728 + seg * 576 + hg * 72 + cc * 8;
      } else {
        src = wo + (size_t)(kap * 32 + row) * D_ + grp * 64 + ch * 8;
      }
      v[i] = *(const u32x4*)src;
    }
#pragma unroll
    for (int i = 0; i < 4; ++i) *(u32x4*)(img + (i * 8 + (lane >> 3)) * 160 + (lane & 7) * 16) = v[i];
    asm volatile("s_waitcnt lgkmcnt(0)" ::: "memory");
    const unsigned a0 = (unsigned)(uintptr_t)(LAS char*)img + (8 * (lane >> 4) + ((lane & 15) >> 2)) * 160 + 8 * (lane & 3);
#pragma unroll
    for (int f = 0; f < 4; ++f) {
      const bf16x8 fr = tr_frag(a0 + f * 32, a0 + f * 32 + 4 * 160);
      const int nf = grp * 4 + f;
      if (isq) { if (nf < 14) *(bf16x8*)(wqf + ((size_t)((hg * 14 + nf) * 18 + kap) * 64 + lane) * 8) = fr; }
      else *(bf16x8*)(wof + ((size_t)(nf * 18 + kap) * 64 + lane) * 8) = fr;
    }
    return;
  }
  b -= NB_PACK;
  u32x4* fl = (u32x4*)(ws + WS_FLAGS);
  for (int i = b * 256 + tid; i < FLAG_WORDS / 4; i += NB_FLAG * 256) fl[i] = (u32x4){0u, 0u, 0u, 0u};
}

namespace mlp {
constexpr int NBUF = 8, SLOT = 18432, PF = 3, NPRE = 5;
constexpr int RING_BYTES = NBUF * SLOT;
constexpr int NU1 = 21, NU2 = 9, NU = NU1 + NU2;
constexpr int NS1 = 324, NS2 = 162, NS = NS1 + NS2;
enum { OP_NONE = 0, OP_W, OP_B, OP_POLL };
constexpr int slot_of(int U) { return (U + 3) % NBUF; }
constexpr int unit_chunk(int U) { return U < 9 ? 0 : U < 15 ? 1 : U < 21 ? 2 : 3; }
constexpr int unit_np(int U) { return unit_chunk(U) == 2 ? 15 : 18; }
constexpr int unit_nk(int U) { int c = unit_chunk(U); return (c == 0 || c == 3) ? 2 : 3; }
constexpr int unit_k0(int U) { return U < 9 ? 2 * U : U < 15 ? 3 * (U - 9) : U < 21 ? 3 * (U - 15) : 2 * (U - 21); }
constexpr int chunk_nt(int c) { return c == 0 ? 4 : c == 1 ? 3 : 2; }
constexpr int chunk_t0(int c) { return c == 0 ? 0 : c == 1 ? 4 : 7; }
constexpr int tile_set(int i) { return i < 4 ? i : i < 7 ? i - 4 : i == 7 ? 3 : 0; }
constexpr int run_a0(int l) { return l == 0 ? 0 : l == 1 ? 3 : l == 2 ? 2 : 5; }
constexpr int run_c1(int l) { return l == 0 ? 4 : l == 1 ? 0 : l == 2 ? 6 : 0; }
constexpr int run_e0(int l) { return l == 0 ? 7 : l == 1 ? 7 : l == 2 ? 0 : 3; }
constexpr int k2_row(int k2, int t) {
  if (k2 < 8) return (k2 >> 1) * 144 + 16 * (run_a0(k2 >> 1) + 2 * (k2 & 1) + t);
  if (k2 < 12) return (k2 - 8) * 144 + 16 * (run_c1(k2 - 8) + t);
  if (k2 < 14) return (2 * (k2 - 12) + t) * 144 + 16 * (run_c1(2 * (k2 - 12) + t) + 2);
  return (k2 - 14) * 144 + 16 * (run_e0(k2 - 14) + t);
}
constexpr int k2_hoff(int k2, int t) {
  if (k2 < 8) return (k2 >> 1) * 4608 + (k2 & 1) * 1024;
  if (k2 < 12) return (k2 - 8) * 4608 + 2048;
  if (k2 < 14) return (2 * (k2 - 12) + t) * 4608 + 4096;
  return (k2 - 14) * 4608 + 3072;
}
struct Sched {
  short unit[NS], ub[NS], kl[NS], kk[NS], mat[NS], tile[NS], first[NS];
  short blk_u0[NS], blk_nu[NS], bar_wait[NS];
  short op_t[NS], op_a[NS], op_b[NS];
  short pro_n, pro_a[16], pro_b[16];
  short bwait[NS];
  short eA[NS], eB[NS], eC[NS];
  short pub[NS], pub_wait[NS];
  short chk[NS], chk_wait[NS];
  bool ok;
};
constexpr Sched make_sched() {
  Sched s{};
  s.ok = true;
  int G = 0;
  int blocks[26][2] = {{0,1},{1,1},{2,1},{3,1},{4,1},{5,1},{6,1},{7,2},{9,2},{11,1},{12,1},{13,2},{15,2},{17,1},{18,1},{19,2},
                       {21,1},{22,1},{23,1},{24,1},{25,1},{26,1},{27,1},{28,1},{29,1},{-1,0}};
  int blk_of_unit[NU] = {};
  for (int i = 0; i < NS; ++i) { s.blk_u0[i] = -1; s.bar_wait[i] = -1; s.bwait[i] = -1; s.eA[i] = -1; s.eB[i] = -1; s.eC[i] = -1; }
  for (int B = 0; blocks[B][0] >= 0; ++B) {
    int u0 = blocks[B][0], nu = blocks[B][1], c = unit_chunk(u0);
    s.blk_u0[G] = (short)u0; s.blk_nu[G] = (short)nu;
    for (int k = 0; k < nu; ++k) blk_of_unit[u0 + k] = B;
    if (c < 3) {
      for (int t = 0; t < chunk_nt(c); ++t)
        for (int k = 0; k < nu; ++k)
          for (int kl = 0; kl < unit_nk(u0); ++kl)
            for (int m = 0; m < 2; ++m) {
              s.unit[G] = (short)(u0 + k); s.ub[G] = (short)k; s.kl[G] = (short)kl; s.kk[G] = (short)(unit_k0(u0 + k) + kl);
              s.mat[G] = (short)m; s.tile[G] = (short)(chunk_t0(c) + t); s.first[G] = (short)(s.kk[G] == 0); ++G;
            }
    } else {
      for (int kl = 0; kl < 2; ++kl)
        for (int nf = 0; nf < 9; ++nf) {
          s.unit[G] = (short)u0; s.ub[G] = 0; s.kl[G] = (short)kl; s.kk[G] = (short)(unit_k0(u0) + kl);
          s.mat[G] = 0; s.tile[G] = (short)nf; s.first[G] = (short)(s.kk[G] == 0); ++G;
        }
    }
  }
  int store_at[NS] = {}, last_store[3] = {};
  int prev = 0;
  for (int i = 0; i < 9; ++i) {
    int R = 0, D = 100000;
    for (int g = 0; g < NS1; ++g) if (s.tile[g] == i) R = g + 1;
    for (int g = R; g < NS1; ++g) if (tile_set(s.tile[g]) == tile_set(i)) { D = g; break; }
    int st = R > prev ? R : prev;
    int sp = (st + 14 < D) ? 2 : 1;
    if (st + sp * 7 >= D) s.ok = false;
    for (int j = 0; j < 8; ++j) {
      int g = st + sp * j;
      s.eA[g] = (short)(i * 8 + j); s.eB[g + 1] = (short)(i * 8 + j); s.eC[g + 2] = (short)(i * 8 + j);
      bool second = (i == 1 || i == 3 || i == 5 || i == 6 || i == 8);
      if (second && (j == 3 || j == 7)) { store_at[g + 2] += 1; last_store[i < 4 ? 0 : i < 7 ? 1 : 2] = g + 2; }
    }
    prev = st + sp * 8;
  }
  const int poll_at[3] = {236, 332, 400}, chk_at[3] = {254, 348, 414};
  for (int c = 0; c < 3; ++c) { s.pub[last_store[c] + 14] = (short)(c + 1); s.chk[chk_at[c]] = (short)(c + 1); }
  int xlast[18] = {};
  for (int g = 0; g < NS1; ++g) xlast[s.kk[g]] = g;
  int bt[96] = {}, ba[96] = {}, bb[96] = {}, be[96] = {}, nb = 0;
  for (int k = 0; k < 18; ++k) for (int m = 0; m < 2; ++m) { bt[nb] = OP_B; ba[nb] = k; bb[nb] = m; be[nb] = 0; ++nb; }
  for (int c = 0; c < 3; ++c) {
    bt[nb] = OP_POLL; ba[nb] = c; bb[nb] = 0; be[nb] = poll_at[c]; ++nb;
    int k0 = c == 0 ? 0 : c == 1 ? 8 : 14, k1 = c == 0 ? 8 : c == 1 ? 14 : 18;
    for (int k = k0; k < k1; ++k) for (int m = 0; m < 2; ++m) {
      int e = chk_at[c] + 1; if (xlast[k] + 1 > e) e = xlast[k] + 1;
      bt[nb] = OP_B; ba[nb] = 18 + k; bb[nb] = m; be[nb] = e; ++nb;
    }
  }
  int wa[96] = {}, wb[96] = {}, nw = 0;
  for (int U = NPRE; U < NU; ++U) for (int j = 0; j < (unit_np(U) + 7) / 8; ++j) { wa[nw] = U; wb[nw] = j; ++nw; }
  int n = 0, wpos[NU] = {}, bpos[36] = {}, wstep[NU] = {}, bstep[36] = {}, pollpos = 0, storepos = 0;
  bool wdone[NU] = {}, bdone[36] = {};
  for (int U = 0; U < NPRE; ++U) wdone[U] = true;
  int bi = 0, wi = 0;
  while (bi < 8) { s.pro_a[s.pro_n] = (short)ba[bi]; s.pro_b[s.pro_n] = (short)bb[bi]; ++s.pro_n; ++n; bpos[ba[bi]] = n; if (bb[bi]) bdone[ba[bi]] = true; ++bi; }
  s.bar_wait[0] = (short)n;
  int bars_done = 0, last_issue = -10;
  bool bseen[36] = {};
  for (int g = 0; g < NS; ++g) {
    int gn = g + PF;
    if (gn < NS && s.blk_u0[gn] >= 0) {
      int need = 0;
      for (int k = 0; k < s.blk_nu[gn]; ++k) { int U = s.blk_u0[gn] + k; if (!wdone[U]) s.ok = false; if (wpos[U] > need) need = wpos[U]; }
      s.bar_wait[gn] = (short)(n - need); bars_done = blk_of_unit[s.blk_u0[gn]];
    }
    if (g - last_issue >= 2) {
      bool wready = wi < nw && !(wa[wi] - NBUF >= 0 && blk_of_unit[wa[wi] - NBUF] > bars_done - 2);
      bool wurgent = wready && wa[wi] <= s.unit[g] + 3;
      bool bready = bi < nb && g >= be[bi];
      if (bready && !wurgent) {
        s.op_t[g] = (short)bt[bi]; s.op_a[g] = (short)ba[bi]; s.op_b[g] = (short)bb[bi];
        if (bt[bi] == OP_POLL) { ++n; pollpos = n; }
        else { n += (ba[bi] == 30 || ba[bi] == 31) ? 2 : 1; bpos[ba[bi]] = n; bstep[ba[bi]] = g; if (bb[bi]) bdone[ba[bi]] = true; }
        ++bi; last_issue = g;
      } else if (wready) {
        s.op_t[g] = OP_W; s.op_a[g] = (short)wa[wi]; s.op_b[g] = (short)wb[wi];
        if ((wb[wi] + 1) * 8 <= unit_np(wa[wi])) ++n;
        wpos[wa[wi]] = n; wstep[wa[wi]] = g;
        if ((wb[wi] + 1) * 8 >= unit_np(wa[wi])) wdone[wa[wi]] = true;
        ++wi; last_issue = g;
      }
    }
    if (s.chk[g]) s.chk_wait[g] = (short)(n - pollpos);
    int bk = (g >= NS1 ? 18 : 0) + s.kk[g];
    if (!bseen[bk]) { bseen[bk] = true; if (!bdone[bk]) s.ok = false; s.bwait[g] = (short)(n - bpos[bk]); }
    if (store_at[g]) { n += store_at[g]; storepos = n; }
    if (s.pub[g]) { s.pub_wait[g] = (short)(n - storepos); ++n; }
  }
  for (int g = 0; g < NS; ++g) if (s.bar_wait[g] > 63 || s.bwait[g] > 63 || s.pub_wait[g] > 63 || s.chk_wait[g] > 63) s.ok = false;
  (void)wstep; (void)bstep;
  return s;
}
constexpr Sched SC = make_sched();
static_assert(SC.ok, "schedule");
}

namespace p1 {
constexpr int XS = 0;
constexpr int MODS = 92160;
constexpr int QP = 456;
constexpr int OS = 40960;
constexpr int OP = 160;
}
namespace p2 {
constexpr int OSM = 0;
constexpr int RED = 36864;
constexpr int RST = RED + 9 * 32 * 8;
constexpr int X1S = mlp::RING_BYTES;
}
constexpr int LDS_BYTES = mlp::RING_BYTES + 3 * 512 * 8;

template <int NN, int M0>
DI void p1_gemm(char* smem, const bf16x8 (&wf)[2][18], f32x4 (&acc)[2][5], int lane) {
  const char* xs = smem + p1::XS + lane * 16;
  static_for<18>([&](auto kc) LAMBDA_INLINE {
    constexpr int k = decltype(kc)::value;
    bf16x8 bf[5];
#pragma unroll
    for (int m = M0; m < 5; ++m) bf[m] = *(const bf16x8*)(xs + (k * 5 + m) * 1024);
#pragma unroll
    for (int n = 0; n < NN; ++n)
#pragma unroll
      for (int m = M0; m < 5; ++m) acc[n][m] = __builtin_amdgcn_mfma_f32_16x16x32_bf16(wf[n][k], bf[m], acc[n][m], 0, 0, 0);
  });
}

__global__ __launch_bounds__(512, 2) void k_main(const u16* __restrict__ x, const u16* __restrict__ w1, const u16* __restrict__ w3,
                                                 const u16* __restrict__ w2, char* __restrict__ ws, u16* __restrict__ out) {
  using namespace mlp;
  extern __shared__ __attribute__((aligned(16))) char smem[];
  const int tid = threadIdx.x, lane = tid & 63;
  const int wave = __builtin_amdgcn_readfirstlane(tid >> 6);
  const int b = blockIdx.x, xcd = b & 7, idx = b >> 3;
  const int mt = idx >> 2, ftl = idx & 3;
  const unsigned lds0 = (unsigned)(uintptr_t)(LAS char*)smem;
  const float* modp = (const float*)(ws + WS_MODP);
  unsigned* flags = (unsigned*)(ws + WS_FLAGS);
  STAMP_AT(0);
  const int a0 = ftl == 0 ? 0 : ftl == 1 ? 3 : ftl == 2 ? 2 : 5;
  const int c1 = ftl == 0 ? 4 : ftl == 1 ? 0 : ftl == 2 ? 6 : 0;
  const int e0 = ftl == 0 ? 7 : ftl == 1 ? 7 : ftl == 2 ? 0 : 3;
  const int fbase = (xcd * 4 + ftl) * 144;

  const char* sp[3];
  unsigned so[3];
  auto stage_setup = [&](auto cc_) LAMBDA_INLINE {
    constexpr int c = decltype(cc_)::value;
    const int lane = lane_now();
#pragma unroll
    for (int j = 0; j < 3; ++j) {
      int i = wave + 8 * j;
      if (c == 2) { if (i > 14) i = 14; } else { if (i > 17) i = 17; }
      const int ci = i * 64 + lane;
      if constexpr (c == 0) {
        int row = ci / 18, cc = ci % 18; if (cc > 15) cc = 15;
        sp[j] = (const char*)((cc >= 8 ? w3 : w1) + (size_t)row * F_ + fbase + a0 * 16 + (cc & 7) * 8);
      } else if constexpr (c == 1) {
        int row = ci / 12, cc = ci % 12, pos = (cc >> 1) ^ ((row >> 2) & 1);
        sp[j] = (const char*)((pos >= 3 ? w3 : w1) + (size_t)row * F_ + fbase + c1 * 16 + (pos >= 3 ? pos - 3 : pos) * 16 + (cc & 1) * 8);
      } else if constexpr (c == 2) {
        int row = ci / 10, cc = ci % 10; if (cc > 7) cc = 7;
        sp[j] = (const char*)((cc >= 4 ? w3 : w1) + (size_t)row * F_ + fbase + e0 * 16 + (cc & 3) * 8);
      } else {
        int ip = i >= 9 ? i - 9 : i, c2 = ip * 64 + lane, row = c2 / 18, cc = c2 % 18;
        so[j] = (unsigned)(row * D_ + cc * 8) * 2u;
      }
    }
  };
  const u16* w2b = w2 + (size_t)xcd * 576 * D_ + ftl * 144;
  auto stage_round = [&](auto Uc, auto jc) LAMBDA_INLINE {
    constexpr int U = decltype(Uc)::value, j = decltype(jc)::value, c = unit_chunk(U);
    if constexpr (j == 0 && unit_k0(U) == 0) stage_setup(IC(c));
    const unsigned dst = lds0 + slot_of(U) * SLOT + (wave + 8 * j) * 1024;
    constexpr bool all = (j + 1) * 8 <= unit_np(U);
    if (all || wave + 8 * j < unit_np(U)) {
      if constexpr (c < 3) {
        glds16v(sp[j], dst);
      } else {
        constexpr int kA = unit_k0(U), kB = kA + 1;
        const bool h1 = wave + 8 * j >= 9;
        const int r0 = h1 ? k2_row(kB, 0) : k2_row(kA, 0);
        constexpr int dA = k2_row(kA, 1) - k2_row(kA, 0) - 16, dB = k2_row(kB, 1) - k2_row(kB, 0) - 16;
        unsigned vo = so[j];
        if constexpr (dA != 0 || dB != 0) vo += (so[j] >= 16u * D_ * 2u) ? (unsigned)((h1 ? dB : dA) * D_ * 2) : 0u;
        glds16(vo, w2b + (size_t)r0 * D_, dst);
      }
    }
    if constexpr (c < 3) sp[j] += (size_t)unit_nk(U) * 32 * F_ * 2;
  };

  const int g = lane >> 4, m16 = lane & 15;
  bf16x8 wf[2][18];
  {
    using namespace p1;
    const int rt = idx, hg = xcd;
    const float* stat1 = (const float*)(ws + WS_STAT1);
    const int r0 = rt * 64 - 16;
    f32x4 mp[3];
    if (tid < 288) {
#pragma unroll
      for (int i = 0; i < 3; ++i) mp[i] = *(const f32x4*)(modp + i * 3456 + tid * 4);
    }
    u32x4 xv[12];
    float rs[5], nm[5];
#pragma unroll
    for (int m = 0; m < 5; ++m) {
      int row = r0 + m * 16 + m16; if (row < 0) row = 0;
      const u16* xr = x + (size_t)row * D_ + 8 * g;
      xv[2 * m] = *(const u32x4*)(xr + 64 * wave);
      xv[2 * m + 1] = *(const u32x4*)(xr + 64 * wave + 32);
      rs[m] = stat1[2 * row]; nm[m] = stat1[2 * row + 1];
    }
    const int kx = 16 + (wave & 1), mx = wave >> 1;
    float rsx, nmx;
    {
      int row = r0 + mx * 16 + m16; if (row < 0) row = 0;
      xv[10] = *(const u32x4*)(x + (size_t)row * D_ + 8 * g + 32 * kx);
      rsx = stat1[2 * row]; nmx = stat1[2 * row + 1];
      xv[11] = *(const u32x4*)(x + (size_t)(r0 + 64 + m16) * D_ + 8 * g + 32 * kx);
    }
    const int nf0 = wave < 4 ? 4 + 2 * wave : wave < 6 ? 2 * (wave - 4) : 6 + wave;
    {
      const bf16x8* wp = (const bf16x8*)(ws + WS_WQF) + (size_t)(hg * 14 + nf0) * 18 * 64 + lane;
#pragma unroll
      for (int k = 0; k < 18; ++k) {
        wf[0][k] = wp[k * 64];
        if (wave < 6) wf[1][k] = wp[(18 + k) * 64];
      }
    }
    float* mods = (float*)(smem + MODS);
    if (tid < 288) {
      f32x4 v = mp[0] + mp[1] + mp[2];
#pragma unroll
      for (int j = 0; j < 4; ++j) { v[j] = rbf(v[j]); if (tid >= 144) v[j] = rbf(1.f + v[j]); }
      *(f32x4*)(mods + tid * 4) = v;
    }
    lds_barrier();
    auto norm8 = [&](u32x4 v, float r, float n, const f32x4 (&sa)[2], const f32x4 (&aa)[2]) LAMBDA_INLINE {
      u32x4 o;
#pragma unroll
      for (int j = 0; j < 4; ++j) {
        const float lo = bflo(v[j]) * r + n, hi = bfhi(v[j]) * r + n;
        o[j] = pack_bf16(lo * aa[j >> 1][(2 * j) & 3] + sa[j >> 1][(2 * j) & 3], hi * aa[j >> 1][(2 * j + 1) & 3] + sa[j >> 1][(2 * j + 1) & 3]);
      }
      return o;
    };
#pragma unroll
    for (int kk = 0; kk < 3; ++kk) {
      const int kap = kk < 2 ? 2 * wave + kk : kx;
      f32x4 sa[2], aa[2];
      sa[0] = *(const f32x4*)(mods + kap * 32 + 8 * g); sa[1] = *(const f32x4*)(mods + kap * 32 + 8 * g + 4);
      aa[0] = *(const f32x4*)(mods + 576 + kap * 32 + 8 * g); aa[1] = *(const f32x4*)(mods + 576 + kap * 32 + 8 * g + 4);
      if (kk < 2) {
#pragma unroll
        for (int m = 0; m < 5; ++m) *(u32x4*)(smem + XS + (kap * 5 + m) * 1024 + lane * 16) = norm8(xv[2 * m + kk], rs[m], nm[m], sa, aa);
      } else {
        *(u32x4*)(smem + XS + (kap * 5 + mx) * 1024 + lane * 16) = norm8(xv[10], rsx, nmx, sa, aa);
        if (wave < 2) *(u32x4*)(smem + XS + (kap * 5 + 4) * 1024 + lane * 16) = norm8(xv[11], rs[4], nm[4], sa, aa);
      }
    }
    lds_barrier();
    STAMP_AT(1);
    f32x4 acc[2][5] = {};
    if (wave < 4) p1_gemm<2, 0>(smem, wf, acc, lane);
    else if (wave < 6) p1_gemm<2, 1>(smem, wf, acc, lane);
    else p1_gemm<1, 0>(smem, wf, acc, lane);
    lds_barrier();
    STAMP_AT(2);
    {
      const bf16x8* wp = (const bf16x8*)(ws + WS_WOF) + (size_t)(ftl * 9 + wave) * 18 * 64 + lane;
      const bf16x8* wp8 = (const bf16x8*)(ws + WS_WOF) + (size_t)(ftl * 9 + 8) * 18 * 64 + lane;
#pragma unroll
      for (int k = 0; k < 18; ++k) { wf[0][k] = wp[k * 64]; if (wave < 2) wf[1][k] = wp8[k * 64]; }
    }
#pragma unroll
    for (int n = 0; n < 2; ++n)
#pragma unroll
      for (int m = 0; m < 5; ++m) {
        if (n == 1 && wave >= 6) continue;
        u32x2 v = {pack_bf16(acc[n][m][0], acc[n][m][1]), pack_bf16(acc[n][m][2], acc[n][m][3])};
        *(u32x2*)(smem + (m * 16 + m16) * QP + ((nf0 + n) * 16 + 4 * g) * 2) = v;
      }
    lds_barrier();
    {
      typedef __bf16 bf2 __attribute__((ext_vector_type(2)));
      const int rl = (wave >> 1) * 16 + m16, hd = (wave & 1) * 2 + (g & 1), hw = lane >> 5;
      const int grow = rt * 64 + rl;
      const unsigned* qrow = (const unsigned*)(smem + (rl + 16) * QP) + hd * 9;
      unsigned qv[9];
#pragma unroll
      for (int d = 0; d < 9; ++d) qv[d] = qrow[d];
      float lg[4];
#pragma unroll
      for (int w = 0; w < 4; ++w) {
        const int ww = hw * 4 + w;
        const unsigned* kr = (const unsigned*)(smem + (rl + 16 - 2 * ww) * QP) + 36 + hd * 9;
        float a = 0.f;
#pragma unroll
        for (int d = 0; d < 9; ++d) a = __builtin_amdgcn_fdot2_f32_bf16(__builtin_bit_cast(bf2, qv[d]), __builtin_bit_cast(bf2, kr[d]), a, false);
        a *= 0.23570226039551584f;
        lg[w] = (grow - 2 * ww >= 0) ? a : -1e9f;
      }
      float mxv = fmaxf(fmaxf(lg[0], lg[1]), fmaxf(lg[2], lg[3]));
      mxv = fmaxf(mxv, __shfl_xor(mxv, 32));
      float pw[8], sum = 0.f;
#pragma unroll
      for (int w = 0; w < 4; ++w) { lg[w] = __builtin_amdgcn_exp2f((lg[w] - mxv) * 1.44269504089f); sum += lg[w]; }
      sum += __shfl_xor(sum, 32);
      const float inv = __builtin_amdgcn_rcpf(sum);
#pragma unroll
      for (int w = 0; w < 4; ++w) {
        const float mine = lg[w] * inv, other = __shfl_xor(mine, 32);
        pw[w] = hw ? other : mine; pw[4 + w] = hw ? mine : other;
      }
      float o[10] = {};
#pragma unroll
      for (int w = 0; w < 8; ++w) {
        const unsigned* vr = (const unsigned*)(smem + (rl + 16 - 2 * w) * QP) + 72 + hd * 9 + hw * 5;
#pragma unroll
        for (int d = 0; d < 5; ++d) { const unsigned v = vr[d]; o[2 * d] += pw[w] * bflo(v); o[2 * d + 1] += pw[w] * bfhi(v); }
      }
      unsigned* orow = (unsigned*)(smem + OS + rl * OP) + hd * 9 + hw * 5;
#pragma unroll
      for (int d = 0; d < 5; ++d) if (d < 4 || hw == 0) orow[d] = pack_bf16(o[2 * d], o[2 * d + 1]);
    }
    lds_barrier();
    STAMP_AT(3);
    const rsrc_t ors = mk_rsrc(ws + WS_OF, S_ * D_ * 2);
#pragma unroll
    for (int it = 0; it < 2; ++it) {
      const int pc = tid + it * 512;
      if (pc < 576) {
        const int row = pc & 63, c = pc >> 6;
        const u32x4 v = *(const u32x4*)(smem + OS + row * OP + c * 16);
        const int c8 = hg * 9 + c, mf = rt * 4 + (row >> 4);
        st16_sc1(ors, (unsigned)(((mf * 18 + (c8 >> 2)) * 64 + (c8 & 3) * 16 + (row & 15)) * 16), 0, v);
      }
    }
    wait_vm<0>();
    lds_barrier();
    if (tid == 0) set_flag(flags + FL_O + rt * 16 + hg, 1u);
    STAMP_AT(4);
  }
  static_for<NPRE * 3>([&](auto ic) LAMBDA_INLINE { stage_round(IC(decltype(ic)::value / 3), IC(decltype(ic)::value % 3)); });

  {
    using namespace p2;
    const int rt = mt * 8 + xcd, nt = ftl;
    u32x2 xr[3]; f32x4 ga[2], sb[2], cb[2];
#pragma unroll
    for (int t = 0; t < 3; ++t) {
      const int nf = t < 2 ? wave : 8, mi = t < 2 ? t : (wave & 1);
      xr[t] = *(const u32x2*)(x + (size_t)(rt * 32 + mi * 16 + m16) * D_ + nt * 144 + nf * 16 + 4 * g);
    }
#pragma unroll
    for (int t = 0; t < 2; ++t) {
      const float* mp = modp + 2 * D_ + nt * 144 + (t ? 8 : wave) * 16 + 4 * g;
      ga[t] = *(const f32x4*)mp + *(const f32x4*)(mp + 3456) + *(const f32x4*)(mp + 2 * 3456);
    }
    wait_flags(flags + FL_O + (rt >> 1) * 16, 8, 1u, lane);
    STAMP_AT(5);
    const rsrc_t ors = mk_rsrc(ws + WS_OF, S_ * D_ * 2);
    u32x4 ov[5];
#pragma unroll
    for (int i = 0; i < 5; ++i) {
      const int f = wave + 8 * i;
      if (f < 36) ov[i] = ld16_sc1(ors, lane * 16, (unsigned)((rt * 36 + f) * 1024));
    }
#pragma unroll
    for (int i = 0; i < 5; ++i) {
      const int f = wave + 8 * i;
      if (f < 36) *(u32x4*)(smem + OSM + f * 1024 + lane * 16) = ov[i];
    }
    lds_barrier();
    STAMP_AT(6);
    f32x4 acc[3] = {};
#pragma unroll
    for (int k = 0; k < 18; ++k) {
      const bf16x8 b0 = *(const bf16x8*)(smem + OSM + k * 1024 + lane * 16), b1 = *(const bf16x8*)(smem + OSM + (18 + k) * 1024 + lane * 16);
      acc[0] = __builtin_amdgcn_mfma_f32_16x16x32_bf16(wf[0][k], b0, acc[0], 0, 0, 0);
      acc[1] = __builtin_amdgcn_mfma_f32_16x16x32_bf16(wf[0][k], b1, acc[1], 0, 0, 0);
      if (wave < 2) acc[2] = __builtin_amdgcn_mfma_f32_16x16x32_bf16(wf[1][k], wave ? b1 : b0, acc[2], 0, 0, 0);
    }
    asm volatile("" ::: "memory");
#pragma unroll
    for (int t = 0; t < 2; ++t) {
      const float* mp = modp + 3 * D_ + nt * 144 + (t ? 8 : wave) * 16 + 4 * g;
      sb[t] = *(const f32x4*)mp + *(const f32x4*)(mp + 3456) + *(const f32x4*)(mp + 2 * 3456);
      cb[t] = *(const f32x4*)(mp + D_) + *(const f32x4*)(mp + D_ + 3456) + *(const f32x4*)(mp + D_ + 2 * 3456);
    }
    float2* red = (float2*)(smem + RED);
    f32x4 xv[3];
#pragma unroll
    for (int t = 0; t < 3; ++t) {
      if (t == 2 && wave >= 2) continue;
      const int nf = t < 2 ? wave : 8, mi = t < 2 ? t : (wave & 1);
      const f32x4 gv = ga[t >> 1];
      u32x2 xb = {pack_bf16(bflo(xr[t][0]) + rbf(gv[0]) * acc[t][0], bfhi(xr[t][0]) + rbf(gv[1]) * acc[t][1]),
                  pack_bf16(bflo(xr[t][1]) + rbf(gv[2]) * acc[t][2], bfhi(xr[t][1]) + rbf(gv[3]) * acc[t][3])};
      *(u32x2*)(smem + X1S + (t * 512 + tid) * 8) = xb;
      f32x4 v = {bflo(xb[0]), bfhi(xb[0]), bflo(xb[1]), bfhi(xb[1])};
      xv[t] = v;
      float s = v[0] + v[1] + v[2] + v[3];
      s += __shfl_xor(s, 16); s += __shfl_xor(s, 32);
      const float mu = s * (1.f / 16.f);
      float q = (v[0] - mu) * (v[0] - mu) + (v[1] - mu) * (v[1] - mu) + (v[2] - mu) * (v[2] - mu) + (v[3] - mu) * (v[3] - mu);
      q += __shfl_xor(q, 16); q += __shfl_xor(q, 32);
      if (g == 0) red[nf * 32 + mi * 16 + m16] = make_float2(mu, q);
    }
    lds_barrier();
    STAMP_AT(7);
    if (wave == 0) {
      GAS u64* stg = (GAS u64*)(flags + FL_ST) + (size_t)rt * 128;
      const int row = lane & 31;
      float mu = 0.f;
#pragma unroll
      for (int nf = 0; nf < 9; ++nf) mu += red[nf * 32 + row].x;
      mu *= (1.f / 9.f);
      float q = 0.f;
#pragma unroll
      for (int nf = 0; nf < 9; ++nf) { const float2 e = red[nf * 32 + row]; q += e.y + 16.f * (e.x - mu) * (e.x - mu); }
      if (lane < 32) __hip_atomic_store(stg + nt * 32 + row, ((u64)(__float_as_uint(q) | 0x80000000u) << 32) | __float_as_uint(mu), RLX_AGENT);
      u64 e0v = 0, e1v = 0;
      for (unsigned spin = 0; spin < SPIN_CAP; ++spin) {
        e0v = __hip_atomic_load(stg + lane, RLX_AGENT);
        e1v = __hip_atomic_load(stg + 64 + lane, RLX_AGENT);
        if (__all((e0v >> 63) && (e1v >> 63))) break;
        __builtin_amdgcn_s_sleep(1);
      }
      const float m0 = __uint_as_float((unsigned)e0v), m1 = __uint_as_float((unsigned)e1v);
      const float q0 = __uint_as_float((unsigned)(e0v >> 32) & 0x7fffffffu), q1 = __uint_as_float((unsigned)(e1v >> 32) & 0x7fffffffu);
      float ms = m0 + m1; ms += __shfl_xor(ms, 32);
      const float mean = 0.25f * ms;
      float qs = q0 + q1 + 144.f * ((m0 - mean) * (m0 - mean) + (m1 - mean) * (m1 - mean));
      qs += __shfl_xor(qs, 32);
      const float rstd = __builtin_amdgcn_rsqf(qs * (1.f / D_) + 1e-5f);
      if (lane < 32) ((float2*)(smem + RST))[row] = make_float2(rstd, -mean * rstd);
    }
    lds_barrier();
    STAMP_AT(8);
    const rsrc_t hrs2 = mk_rsrc(ws + WS_H2F, S_ * D_ * 2);
#pragma unroll
    for (int t = 0; t < 3; ++t) {
      if (t == 2 && wave >= 2) continue;
      const int nf = t < 2 ? wave : 8, mi = t < 2 ? t : (wave & 1);
      const float2 st = ((const float2*)(smem + RST))[mi * 16 + m16];
      float h[4];
#pragma unroll
      for (int j = 0; j < 4; ++j) h[j] = (xv[t][j] * st.x + st.y) * rbf(1.f + rbf(cb[t >> 1][j])) + rbf(sb[t >> 1][j]);
      const int nfg = nt * 9 + nf;
      u32x2 o = {pack_bf16(h[0], h[1]), pack_bf16(h[2], h[3])};
      st8_sc1(hrs2, lane * 16, (unsigned)(((rt * 2 + mi) * 18 + (nfg >> 1)) * 1024 + (nfg & 1) * 8), o);
    }
    wait_vm<0>();
    lds_barrier();
    if (tid == 0) set_flag(flags + FL_H2 + rt * 16 + nt, 1u);
    STAMP_AT(9);
  }

  bf16x8 xf[18][2];
  const int mf0 = (mt * 8 + wave) * 2;
  const char* xb = ws + WS_H2F + (size_t)mf0 * 18 * 1024;
  const char* hb = ws + WS_HF + ((size_t)mf0 * 8 + xcd) * 18432;
  const unsigned* hfl = flags + FL_H + ((xcd * 8 + mt) * 8 + wave) * 16;
  const unsigned l16 = lane * 16;
  unsigned flagv;
  auto bload = [&](auto ac, auto mc) LAMBDA_INLINE {
    constexpr int a = decltype(ac)::value, mi = decltype(mc)::value;
    if constexpr (a < 18) {
      constexpr int o = (mi * 18 + a) * 1024;
      gload16_sc1<o % 4096>(xf[a][mi], l16, xb + (o / 4096) * 4096);
    } else {
      constexpr int k2 = a - 18;
      if constexpr (k2 == 12 || k2 == 13) {
        u32x2 lo, hi;
        const unsigned l8 = lane_now() * 8;
        gload8_sc1(lo, l8, hb + mi * 8 * 18432 + k2_hoff(k2, 0));
        gload8_sc1(hi, l8, hb + mi * 8 * 18432 + k2_hoff(k2, 1));
        xf[k2][mi] = __builtin_bit_cast(bf16x8, __builtin_shufflevector(lo, hi, 0, 1, 2, 3));
      } else {
        gload16_sc1<0>(xf[k2][mi], l16, hb + mi * 8 * 18432 + k2_hoff(k2, 0));
      }
    }
  };
  wait_flags(flags + FL_H2 + (mt * 8 + wave) * 16, 4, 1u, lane);
  STAMP_AT(10);
  static_for<SC.pro_n>([&](auto ic) LAMBDA_INLINE { constexpr int i = decltype(ic)::value; bload(IC(SC.pro_a[i]), IC(SC.pro_b[i])); });

  unsigned ubase[2][2];
  auto block_barrier = [&](auto Gc) LAMBDA_INLINE {
    constexpr int G = decltype(Gc)::value, u0 = SC.blk_u0[G], nu = SC.blk_nu[G], c = unit_chunk(u0);
    sched_fence();
    wait_vm<SC.bar_wait[G]>();
    barrier_raw();
    const int ln = lane_now(), gg = ln >> 4, p = ln & 3, rl = ln >> 2;
#pragma unroll
    for (int k = 0; k < nu; ++k) {
      const unsigned sb = lds0 + slot_of(u0 + k) * SLOT + 8 * p;
      if constexpr (c == 0 || c == 3) ubase[k][0] = sb + rl * 288;
      else if constexpr (c == 1) { ubase[k][0] = sb + rl * 192 + 32 * (gg & 1); ubase[k][1] = sb + rl * 192 + 32 * (1 - (gg & 1)); }
      else ubase[k][0] = sb + rl * 160;
      asm volatile("" : "+v"(ubase[k][0]));
      if constexpr (c == 1) asm volatile("" : "+v"(ubase[k][1]));
    }
    sched_fence();
  };
  auto afrag = [&](auto Gc) LAMBDA_INLINE {
    constexpr int G = decltype(Gc)::value, U = SC.unit[G], c = unit_chunk(U), k = SC.ub[G], kl = SC.kl[G], m = SC.mat[G];
    if constexpr (c == 0) {
      constexpr unsigned o = kl * 32 * 288 + m * 128 + SC.tile[G] * 32;
      return tr_frag(ubase[k][0] + o, ubase[k][0] + o + 16 * 288);
    } else if constexpr (c == 1) {
      constexpr int pos = 3 * m + (SC.tile[G] - 4);
      constexpr unsigned o = kl * 32 * 192 + (pos & ~1) * 32;
      return tr_frag(ubase[k][pos & 1] + o, ubase[k][pos & 1] + o + 16 * 192);
    } else if constexpr (c == 2) {
      constexpr unsigned o = kl * 32 * 160 + m * 64 + (SC.tile[G] - 7) * 32;
      return tr_frag(ubase[k][0] + o, ubase[k][0] + o + 16 * 160);
    } else {
      constexpr unsigned o = kl * 32 * 288 + SC.tile[G] * 32;
      return tr_frag(ubase[k][0] + o, ubase[k][0] + o + 16 * 288);
    }
  };
  f32x4 acc[4][2][2];
  f32x4 acc2[9][2];
  bf16x8 af[PF + 1];
  float ta[2], tp[2], hv[2];
  unsigned pk[2][4];
  const rsrc_t hrs = mk_rsrc(ws + WS_HF, 128 * 8 * 18432);
  const unsigned hso = (unsigned)((mf0 * 8 + xcd) * 18432 + ftl * 4608);
  auto epi = [&](auto Gc) LAMBDA_INLINE {
    constexpr int G = decltype(Gc)::value;
    if constexpr (SC.eC[G] >= 0) {
      constexpr int e = SC.eC[G], i = e >> 3, j = e & 7, mi = j >> 2, r = j & 3;
      hv[r & 1] = tp[e & 1] * ta[e & 1];
      constexpr bool second = (i == 1 || i == 3 || i == 5 || i == 8), single = i == 6;
      if constexpr (r & 1) {
        pk[mi][(second ? 2 : 0) + (r >> 1)] = pack_bf16(hv[0], hv[1]);
        asm volatile("" : "+v"(pk[mi][(second ? 2 : 0) + (r >> 1)]));
      } else {
        asm volatile("" : "+v"(hv[0]));
      }
      if constexpr (r == 3 && second) {
        constexpr int pr = i == 1 ? 0 : i == 3 ? 1 : i == 5 ? 2 : 3;
        u32x4 v = {pk[mi][0], pk[mi][1], pk[mi][2], pk[mi][3]};
        st16_sc1(hrs, l16, hso + mi * 8 * 18432 + pr * 1024, v);
      }
      if constexpr (r == 3 && single) {
        u32x2 v = {pk[mi][0], pk[mi][1]};
        st8_sc1(hrs, lane_now() * 8, hso + mi * 8 * 18432 + 4096, v);
      }
    }
    if constexpr (SC.eB[G] >= 0) {
      constexpr int e = SC.eB[G];
      ta[e & 1] = __builtin_amdgcn_rcpf(1.f + ta[e & 1]);
      asm volatile("" : "+v"(ta[e & 1]));
    }
    if constexpr (SC.eA[G] >= 0) {
      constexpr int e = SC.eA[G], i = e >> 3, j = e & 7, mi = j >> 2, r = j & 3, st = tile_set(i);
      const float gv = acc[st][0][mi][r];
      tp[e & 1] = gv * acc[st][1][mi][r];
      ta[e & 1] = __builtin_amdgcn_exp2f(gv * -1.44269504089f);
      asm volatile("" : "+v"(ta[e & 1]), "+v"(tp[e & 1]));
    }
  };

  block_barrier(IC(0));
  static_for<PF>([&](auto ic) LAMBDA_INLINE { constexpr int G = decltype(ic)::value; af[G % (PF + 1)] = afrag(IC(G)); });
  static_for<NS>([&](auto ic) LAMBDA_INLINE {
    constexpr int G = decltype(ic)::value, GN = G + PF, kk = SC.kk[G];
    if constexpr (G == NS1) STAMP_AT(11);
    if constexpr (GN < NS) {
      if constexpr (SC.blk_u0[GN] >= 0) block_barrier(IC(GN));
    }
    if constexpr (SC.op_t[G] == OP_W) stage_round(IC(SC.op_a[G]), IC(SC.op_b[G]));
    if constexpr (SC.op_t[G] == OP_B) bload(IC(SC.op_a[G]), IC(SC.op_b[G]));
    if constexpr (SC.op_t[G] == OP_POLL) gload4_sc1(flagv, (lane_now() & 3) * 4, hfl);
    if constexpr (SC.chk[G] != 0) {
      asm volatile("s_waitcnt vmcnt(%1)" : "+v"(flagv) : "i"(SC.chk_wait[G]) : "memory");
      if (!__all(flagv >= (unsigned)SC.chk[G])) wait_flags(hfl, 4, (unsigned)SC.chk[G], lane_now());
    }
    if constexpr (SC.bwait[G] >= 0)
      asm volatile("s_waitcnt vmcnt(%2)" : "+v"(xf[kk][0]), "+v"(xf[kk][1]) : "i"(SC.bwait[G]) : "memory");
    const f32x4 z = {};
    if constexpr (G < NS1) {
      constexpr int st = tile_set(SC.tile[G]), m = SC.mat[G];
      acc[st][m][0] = __builtin_amdgcn_mfma_f32_16x16x32_bf16(af[G % (PF + 1)], xf[kk][0], SC.first[G] ? z : acc[st][m][0], 0, 0, 0);
      af[GN % (PF + 1)] = afrag(IC(GN));
      acc[st][m][1] = __builtin_amdgcn_mfma_f32_16x16x32_bf16(af[G % (PF + 1)], xf[kk][1], SC.first[G] ? z : acc[st][m][1], 0, 0, 0);
    } else {
      constexpr int nf = SC.tile[G];
      acc2[nf][0] = __builtin_amdgcn_mfma_f32_16x16x32_bf16(af[G % (PF + 1)], xf[kk][0], SC.first[G] ? z : acc2[nf][0], 0, 0, 0);
      if constexpr (GN < NS) af[GN % (PF + 1)] = afrag(IC(GN));
      acc2[nf][1] = __builtin_amdgcn_mfma_f32_16x16x32_bf16(af[G % (PF + 1)], xf[kk][1], SC.first[G] ? z : acc2[nf][1], 0, 0, 0);
    }
    epi(IC(G));
    if constexpr (SC.pub[G] != 0) {
      wait_vm<SC.pub_wait[G]>();
      if (lane_now() == 0) set_flag((unsigned*)hfl + ftl, (unsigned)SC.pub[G]);
    }
    sched_fence();
  });
  STAMP_AT(12);

  {
    const int ln = lane_now(), gg = ln >> 4, mm = ln & 15;
    const rsrc_t prs = mk_rsrc(ws + WS_PART, 64 * 32 * 9 * 1024);
    const unsigned tile = (unsigned)(mt * 4 + ftl);
#pragma unroll
    for (int nf = 0; nf < 9; ++nf) {
      u32x4 v = {pack_bf16(acc2[nf][0][0], acc2[nf][0][1]), pack_bf16(acc2[nf][0][2], acc2[nf][0][3]),
                 pack_bf16(acc2[nf][1][0], acc2[nf][1][1]), pack_bf16(acc2[nf][1][2], acc2[nf][1][3])};
      st16_sc1(prs, ln * 16, (((wave * 8 + xcd) * 32 + tile) * 9 + nf) * 1024, v);
    }
    f32x4 gb[2];
#pragma unroll
    for (int t = 0; t < 2; ++t) {
      const float* mp = modp + 5 * D_ + ftl * 144 + (t ? 8 : wave) * 16 + 4 * gg;
      gb[t] = *(const f32x4*)mp + *(const f32x4*)(mp + 3456) + *(const f32x4*)(mp + 2 * 3456);
    }
    wait_vm<0>();
    if (ln == 0) set_flag(flags + FL_P + (tile * 8 + wave) * 16 + xcd, 1u);
    STAMP_AT(13);
    wait_flags(flags + FL_P + (tile * 8 + xcd) * 16, 8, 1u, ln);
    STAMP_AT(14);
    u32x4 pv[8]; u32x2 pe[8];
#pragma unroll
    for (int s = 0; s < 8; ++s) {
      pv[s] = ld16_sc1(prs, ln * 16, (((xcd * 8 + s) * 32 + tile) * 9 + wave) * 1024);
      if (wave < 2) pe[s] = ld8_sc1(prs, ln * 16 + wave * 8, (((xcd * 8 + s) * 32 + tile) * 9 + 8) * 1024);
    }
    float y[3][4] = {};
#pragma unroll
    for (int s = 0; s < 8; ++s) {
#pragma unroll
      for (int t = 0; t < 2; ++t) {
        y[t][0] += bflo(pv[s][2 * t]); y[t][1] += bfhi(pv[s][2 * t]); y[t][2] += bflo(pv[s][2 * t + 1]); y[t][3] += bfhi(pv[s][2 * t + 1]);
      }
      if (wave < 2) { y[2][0] += bflo(pe[s][0]); y[2][1] += bfhi(pe[s][0]); y[2][2] += bflo(pe[s][1]); y[2][3] += bfhi(pe[s][1]); }
    }
#pragma unroll
    for (int t = 0; t < 3; ++t) {
      if (t == 2 && wave >= 2) continue;
      const int nf = t < 2 ? wave : 8, mi = t < 2 ? t : (wave & 1);
      const u32x2 x1 = *(const u32x2*)(smem + p2::X1S + (t * 512 + wave * 64 + ln) * 8);
      const f32x4 gv = gb[t >> 1];
      u32x2 o = {pack_bf16(bflo(x1[0]) + rbf(gv[0]) * y[t][0], bfhi(x1[0]) + rbf(gv[1]) * y[t][1]),
                 pack_bf16(bflo(x1[1]) + rbf(gv[2]) * y[t][2], bfhi(x1[1]) + rbf(gv[3]) * y[t][3])};
      *(u32x2*)(out + (size_t)((mt * 8 + xcd) * 32 + mi * 16 + mm) * D_ + ftl * 144 + nf * 16 + 4 * gg) = o;
    }
  }
  STAMP_AT(15);
}

extern "C" void kernel_launch(void* const* d_in, const int* in_sizes, int n_in, void* d_out, int out_size, void* d_ws,
                              size_t ws_size, hipStream_t stream) {
  const u16* x = (const u16*)d_in[0];
  const u16* wqkv = (const u16*)d_in[1];
  const u16* wo = (const u16*)d_in[2];
  const u16* w1 = (const u16*)d_in[4];
  const u16* w3 = (const u16*)d_in[5];
  const u16* w2 = (const u16*)d_in[6];
  const u16* c = (const u16*)d_in[7];
  const u16* wmod = (const u16*)d_in[8];
  (void)hipFuncSetAttribute((const void*)k_main, hipFuncAttributeMaxDynamicSharedMemorySize, LDS_BYTES);
  k_pre<<<k0::NB, 256, 0, stream>>>(x, wqkv, wo, c, wmod, (char*)d_ws);
  k_main<<<256, 512, LDS_BYTES, stream>>>(x, w1, w3, w2, (char*)d_ws, (u16*)d_out);
}
```

```cpp
#include <hip/hip_runtime.h>
#include <hip/hip_bf16.h>
#include <stdint.h>
#include <utility>

typedef unsigned short u16;
typedef unsigned long long u64;
typedef short bf16x8 __attribute__((ext_vector_type(8)));
typedef short s16x4 __attribute__((ext_vector_type(4)));
typedef float f32x4 __attribute__((ext_vector_type(4)));
typedef float f32x16 __attribute__((ext_vector_type(16)));
typedef unsigned u32x4 __attribute__((ext_vector_type(4)));
typedef unsigned u32x2 __attribute__((ext_vector_type(2)));
#define LAS __attribute__((address_space(3)))
#define GAS __attribute__((address_space(1)))
#define DI __device__ __forceinline__
#define LAMBDA_INLINE __attribute__((always_inline))
#define IC(x) std::integral_constant<int, (x)>{}
#define RLX_AGENT __ATOMIC_RELAXED, __HIP_MEMORY_SCOPE_AGENT

constexpr int S_ = 2048, D_ = 576, F_ = 4608;

#define STAMP_AT(i) do {} while (0)

template <int... Is, class Fn> DI void static_for_impl(std::integer_sequence<int, Is...>, Fn&& f) { (f(std::integral_constant<int, Is>{}), ...); }
template <int N, class Fn> DI void static_for(Fn&& f) { static_for_impl(std::make_integer_sequence<int, N>{}, f); }

DI float bf2f(u16 v) { return __uint_as_float(((unsigned)v) << 16); }
DI float bflo(unsigned v) { return __uint_as_float(v << 16); }
DI float bfhi(unsigned v) { return __uint_as_float(v & 0xffff0000u); }
DI unsigned pack_bf16(float lo, float hi) {
  typedef __bf16 bf2 __attribute__((ext_vector_type(2)));
  typedef float f2 __attribute__((ext_vector_type(2)));
  f2 v = {lo, hi};
  bf2 r = __builtin_convertvector(v, bf2);
  return __builtin_bit_cast(unsigned, r);
}
DI float rbf(float f) { return __uint_as_float(pack_bf16(f, 0.f) << 16); }
DI float silu_f(float g) { return g * __builtin_amdgcn_rcpf(1.f + __builtin_amdgcn_exp2f(g * -1.44269504089f)); }

template <int N> DI void wait_vm() { asm volatile("s_waitcnt vmcnt(%0)" ::"i"(N) : "memory"); }
DI void barrier_raw() { asm volatile("" ::: "memory"); __builtin_amdgcn_s_barrier(); asm volatile("" ::: "memory"); }
DI void lds_barrier() { asm volatile("s_waitcnt lgkmcnt(0)" ::: "memory"); __builtin_amdgcn_s_barrier(); asm volatile("" ::: "memory"); }
DI void sched_fence() { __builtin_amdgcn_sched_barrier(0); }
DI int lane_now() {
  int l = __builtin_amdgcn_mbcnt_hi(~0u, __builtin_amdgcn_mbcnt_lo(~0u, 0u));
  asm volatile("" : "+v"(l));
  return l;
}

DI void glds16(unsigned voff, const void* sbase, unsigned lds_dst) {
  unsigned keep;
  asm volatile("s_mov_b32 %0, m0\n\ts_mov_b32 m0, %3\n\ts_nop 0\n\tglobal_load_lds_dwordx4 %1, %2\n\ts_mov_b32 m0, %0"
               : "=&s"(keep) : "v"(voff), "s"(sbase), "s"(lds_dst) : "memory");
}
DI void glds16v(const void* vaddr, unsigned lds_dst) {
  unsigned keep;
  asm volatile("s_mov_b32 %0, m0\n\ts_mov_b32 m0, %2\n\ts_nop 0\n\tglobal_load_lds_dwordx4 %1, off\n\ts_mov_b32 m0, %0"
               : "=&s"(keep) : "v"(vaddr), "s"(lds_dst) : "memory");
}
template <int IMM> DI void gload16_sc1(bf16x8& dst, unsigned voff, const void* sbase) {
  asm volatile("global_load_dwordx4 %0, %1, %2 offset:%3 sc1" : "=v"(dst) : "v"(voff), "s"(sbase), "i"(IMM) : "memory");
}
DI void gload8_sc1(u32x2& dst, unsigned voff, const void* sbase) {
  asm volatile("global_load_dwordx2 %0, %1, %2 sc1" : "=v"(dst) : "v"(voff), "s"(sbase) : "memory");
}
DI void gload4_sc1(unsigned& dst, unsigned voff, const void* sbase) {
  asm volatile("global_load_dword %0, %1, %2 sc1" : "=v"(dst) : "v"(voff), "s"(sbase) : "memory");
}
DI s16x4 tr_read(unsigned addr) { return __builtin_amdgcn_ds_read_tr16_b64_v4i16((LAS s16x4*)(uintptr_t)addr); }
DI bf16x8 tr_frag(unsigned a0, unsigned a1) {
  s16x4 lo = tr_read(a0), hi = tr_read(a1);
  return __builtin_shufflevector(lo, hi, 0, 1, 2, 3, 4, 5, 6, 7);
}
typedef __amdgpu_buffer_rsrc_t rsrc_t;
DI rsrc_t mk_rsrc(const void* p, unsigned bytes) { return __builtin_amdgcn_make_buffer_rsrc((void*)p, 0, bytes, 0x00020000); }
DI void st16_sc1(rsrc_t r, unsigned voff, unsigned soff, u32x4 v) {
  __builtin_amdgcn_raw_buffer_store_b128(v, r, voff, soff, 16);
  asm volatile("s_nop 1" ::"v"(v) : "memory");
}
DI void st8_sc1(rsrc_t r, unsigned voff, unsigned soff, u32x2 v) { __builtin_amdgcn_raw_buffer_store_b64(v, r, voff, soff, 16); }
DI u32x4 ld16_sc1(rsrc_t r, unsigned voff, unsigned soff) { return __builtin_amdgcn_raw_buffer_load_b128(r, voff, soff, 16); }
DI u32x2 ld8_sc1(rsrc_t r, unsigned voff, unsigned soff) { return __builtin_amdgcn_raw_buffer_load_b64(r, voff, soff, 16); }

constexpr unsigned SPIN_CAP = 1u << 15;
DI void wait_flags(const unsigned* f, int n, unsigned need, int lane) {
  const GAS unsigned* p = (const GAS unsigned*)f + (lane < n ? lane : 0);
  for (unsigned spin = 0; spin < SPIN_CAP; ++spin) {
    const unsigned v = __hip_atomic_load(p, RLX_AGENT);
    if (__all(v >= need)) break;
    __builtin_amdgcn_s_sleep(1);
  }
  asm volatile("" ::: "memory");
}
DI void set_flag(unsigned* f, unsigned v) { __hip_atomic_store((GAS unsigned*)f, v, RLX_AGENT); }

constexpr size_t WS_MODP = 0;
constexpr size_t WS_STAT1 = 65536;
constexpr size_t WS_FLAGS = 131072;
constexpr size_t WS_OF = 1u << 20;
constexpr size_t WS_H2F = 4u << 20;
constexpr size_t WS_WQF = 8u << 20;
constexpr size_t WS_WOF = 12u << 20;
constexpr size_t WS_HF = 16u << 20;
constexpr size_t WS_PART = 40u << 20;
constexpr int FL_O = 0;
constexpr int FL_H2 = 512;
constexpr int FL_H = 1536;
constexpr int FL_P = FL_H + 8192;
constexpr int FL_ST = FL_P + 4096;
constexpr int FLAG_WORDS = FL_ST + 16384;

namespace k0 {
constexpr int NB_MOD = 162, NB_STAT = 64, NW_PQ = 8 * 4 * 18, NW_PO = 9 * 18, NB_PACK = (NW_PQ + NW_PO + 3) / 4, NB_FLAG = 8, NB_CODE = 16;
constexpr int NB = NB_CODE + NB_MOD + NB_STAT + NB_PACK + NB_FLAG;
}
DI void k_pre_body(const u16* __restrict__ x, const u16* __restrict__ wqkv, const u16* __restrict__ wo,
                   const u16* __restrict__ cvec, const u16* __restrict__ wmod, char* __restrict__ ws) {
  using namespace k0;
  __shared__ __attribute__((aligned(16))) char sm[4 * 32 * 160];
  const int tid = threadIdx.x, lane = tid & 63, wave = tid >> 6;
  int b = blockIdx.x;
  if (b < NB_CODE) {
    unsigned clo, chi, elo, ehi;
    asm volatile("s_getpc_b64 s[20:21]\n\ts_add_u32 %0, s20, k_main@rel32@lo+4\n\ts_addc_u32 %1, s21, k_main@rel32@hi+12"
                 : "=s"(clo), "=s"(chi)::"s20", "s21", "scc");
    asm volatile("s_getpc_b64 s[20:21]\n\ts_add_u32 %0, s20, k_tail@rel32@lo+4\n\ts_addc_u32 %1, s21, k_tail@rel32@hi+12"
                 : "=s"(elo), "=s"(ehi)::"s20", "s21", "scc");
    const GAS char* code = (const GAS char*)(((u64)chi << 32) | clo);
    const long long len = (long long)(((u64)ehi << 32) | elo) - (long long)(((u64)chi << 32) | clo);
    unsigned acc = 0;
#pragma unroll
    for (int i = 0; i < 4; ++i) {
      const int o = ((i * 2 + (b >> 3)) * 256 + tid) * 64;
      if (o + 4 <= len) acc += *(const GAS unsigned*)(code + o);
    }
    asm volatile("" ::"v"(acc));
    return;
  }
  b -= NB_CODE;
  if (b < NB_MOD) {
    float* modp = (float*)(ws + WS_MODP);
    const int cg = b % 54, ks = b / 54, rg = tid >> 3, cl = tid & 7;
    u32x4 w[6]; float sv[6];
#pragma unroll
    for (int i = 0; i < 6; ++i) {
      const int k = ks * 192 + rg + 32 * i;
      w[i] = *(const u32x4*)(wmod + (size_t)k * 3456 + cg * 64 + cl * 8);
      sv[i] = bf2f(cvec[k]);
    }
    float acc[8] = {};
#pragma unroll
    for (int i = 0; i < 6; ++i) {
      const float s = rbf(silu_f(sv[i]));
#pragma unroll
      for (int j = 0; j < 4; ++j) { acc[2 * j] += s * bflo(w[i][j]); acc[2 * j + 1] += s * bfhi(w[i][j]); }
    }
#pragma unroll
    for (int j = 0; j < 8; ++j) { acc[j] += __shfl_xor(acc[j], 8); acc[j] += __shfl_xor(acc[j], 16); acc[j] += __shfl_xor(acc[j], 32); }
    float* red = (float*)sm;
    if (lane < 8) {
#pragma unroll
      for (int j = 0; j < 8; ++j) red[wave * 64 + lane * 8 + j] = acc[j];
    }
    __syncthreads();
    if (tid < 64) modp[ks * 3456 + cg * 64 + tid] = red[tid] + red[64 + tid] + red[128 + tid] + red[192 + tid];
    return;
  }
  b -= NB_MOD;
  if (b < NB_STAT) {
    float* stat1 = (float*)(ws + WS_STAT1);
    const int row = (b * 4 + wave) * 8 + (lane >> 3), cl = lane & 7;
    u32x4 v[9];
#pragma unroll
    for (int i = 0; i < 9; ++i) v[i] = *(const u32x4*)(x + (size_t)row * D_ + (cl + 8 * i) * 8);
    float s = 0.f;
#pragma unroll
    for (int i = 0; i < 9; ++i)
#pragma unroll
      for (int j = 0; j < 4; ++j) s += bflo(v[i][j]) + bfhi(v[i][j]);
    s += __shfl_xor(s, 1); s += __shfl_xor(s, 2); s += __shfl_xor(s, 4);
    const float mean = s * (1.f / D_);
    float q = 0.f;
#pragma unroll
    for (int i = 0; i < 9; ++i)
#pragma unroll
      for (int j = 0; j < 4; ++j) { const float a = bflo(v[i][j]) - mean, c = bfhi(v[i][j]) - mean; q += a * a + c * c; }
    q += __shfl_xor(q, 1); q += __shfl_xor(q, 2); q += __shfl_xor(q, 4);
    const float rstd = __builtin_amdgcn_rsqf(q * (1.f / D_) + 1e-5f);
    if (cl == 0) { stat1[2 * row] = rstd; stat1[2 * row + 1] = -mean * rstd; }
    return;
  }
  b -= NB_STAT;
  if (b < NB_PACK) {
    u16* wqf = (u16*)(ws + WS_WQF);
    u16* wof = (u16*)(ws + WS_WOF);
    const int t = b * 4 + wave;
    if (t >= NW_PQ + NW_PO) return;
    char* img = sm + wave * (32 * 160);
    const bool isq = t < NW_PQ;
    int kap, grp, hg = 0;
    if (isq) { kap = t % 18; grp = (t / 18) & 3; hg = t / 72; } else { kap = (t - NW_PQ) % 18; grp = (t - NW_PQ) / 18; }
    u32x4 v[4];
#pragma unroll
    for (int i = 0; i < 4; ++i) {
      const int row = i * 8 + (lane >> 3), ch = lane & 7;
      const u16* src;
      if (isq) {
        int c8 = grp * 8 + ch; if (c8 > 26) c8 = 26;
        const int seg = c8 / 9, cc = c8 % 9;
        src = wqkv + (size_t)(kap * 32 + row) * 1728 + seg * 576 + hg * 72 + cc * 8;
      } else {
        src = wo + (size_t)(kap * 32 + row) * D_ + grp * 64 + ch * 8;
      }
      v[i] = *(const u32x4*)src;
    }
#pragma unroll
    for (int i = 0; i < 4; ++i) *(u32x4*)(img + (i * 8 + (lane >> 3)) * 160 + (lane & 7) * 16) = v[i];
    asm volatile("s_waitcnt lgkmcnt(0)" ::: "memory");
    const unsigned a0 = (unsigned)(uintptr_t)(LAS char*)img + (8 * (lane >> 4) + ((lane & 15) >> 2)) * 160 + 8 * (lane & 3);
#pragma unroll
    for (int f = 0; f < 4; ++f) {
      const bf16x8 fr = tr_frag(a0 + f * 32, a0 + f * 32 + 4 * 160);
      const int nf = grp * 4 + f;
      if (isq) { if (nf < 14) *(bf16x8*)(wqf + ((size_t)((hg * 14 + nf) * 18 + kap) * 64 + lane) * 8) = fr; }
      else *(bf16x8*)(wof + ((size_t)(nf * 18 + kap) * 64 + lane) * 8) = fr;
    }
    return;
  }
  b -= NB_PACK;
  u32x4* fl = (u32x4*)(ws + WS_FLAGS);
  for (int i = b * 256 + tid; i < FLAG_WORDS / 4; i += NB_FLAG * 256) fl[i] = (u32x4){0u, 0u, 0u, 0u};
}

__global__ __launch_bounds__(256) void k_pre(const u16* __restrict__ x, const u16* __restrict__ wqkv, const u16* __restrict__ wo,
                                             const u16* __restrict__ cvec, const u16* __restrict__ wmod, char* __restrict__ ws) {
  k_pre_body(x, wqkv, wo, cvec, wmod, ws);
}

namespace mlp {
constexpr int NBUF = 8, SLOT = 18432, PF = 3, NPRE = 8;
constexpr int RING_BYTES = NBUF * SLOT;
constexpr int NU1 = 21, NU2 = 9, NU = NU1 + NU2;
constexpr int NS1 = 324, NS2 = 162, NS = NS1 + NS2;
enum { OP_NONE = 0, OP_W, OP_B, OP_POLL };
#define TA_COST 28
constexpr int CHK_DELAY = 16;
constexpr int slot_of(int U) { return (U + 3) % NBUF; }
constexpr int unit_chunk(int U) { return U < 9 ? 0 : U < 15 ? 1 : U < 21 ? 2 : 3; }
constexpr int unit_np(int U) { return unit_chunk(U) == 2 ? 12 : 18; }
constexpr int unit_nk(int U) { int c = unit_chunk(U); return (c == 0 || c == 3) ? 2 : 3; }
constexpr int unit_k0(int U) { return U < 9 ? 2 * U : U < 15 ? 3 * (U - 9) : U < 21 ? 3 * (U - 15) : 2 * (U - 21); }
constexpr int chunk_nt(int c) { return c == 0 ? 4 : c == 1 ? 3 : 2; }
constexpr int chunk_t0(int c) { return c == 0 ? 0 : c == 1 ? 4 : 7; }
constexpr int tile_set(int i) { return i < 4 ? i : i < 7 ? i - 4 : i == 7 ? 3 : 0; }
constexpr int run_a0(int l) { return l == 0 ? 0 : l == 1 ? 3 : l == 2 ? 2 : 5; }
constexpr int run_c1(int l) { return l == 0 ? 4 : l == 1 ? 0 : l == 2 ? 6 : 0; }
constexpr int run_e0(int l) { return l == 0 ? 7 : l == 1 ? 7 : l == 2 ? 0 : 3; }
constexpr int pos_row(int pos) {
  if (pos < 16) return (pos >> 2) * 144 + 16 * (run_a0(pos >> 2) + (pos & 3));
  if (pos < 28) return ((pos - 16) / 3) * 144 + 16 * (run_c1((pos - 16) / 3) + (pos - 16) % 3);
  return ((pos - 28) >> 1) * 144 + 16 * (run_e0((pos - 28) >> 1) + ((pos - 28) & 1));
}
struct Sched {
  short unit[NS], ub[NS], kl[NS], bi[NS], sub[NS], tile[NS], first[NS];
  short blk_u0[NS], blk_nu[NS], bar_wait[NS];
  short op_t[NS], op_a[NS], op_b[NS];
  short pro_n, pro_a[16], pro_b[16];
  short bwait[NS];
  short eA[NS], eB[NS], eC[NS];
  short pub[NS], pub_wait[NS], chk[NS], chk_wait[NS];
  short pst[NS];
  bool ok;
  short min_lead_b, min_lead_w;
};
constexpr Sched make_sched() {
  Sched s{};
  s.ok = true; s.min_lead_b = 1000; s.min_lead_w = 1000;
  int G = 0;
  int blocks[26][2] = {{0,1},{1,1},{2,1},{3,1},{4,1},{5,1},{6,1},{7,2},{9,2},{11,1},{12,1},{13,2},{15,2},{17,1},{18,1},{19,2},
                       {21,1},{22,1},{23,1},{24,1},{25,1},{26,1},{27,3},{-1,0}};
  int blk_of_unit[NU] = {};
  for (int i = 0; i < NS; ++i) { s.blk_u0[i] = -1; s.bar_wait[i] = -1; s.bwait[i] = -1; s.eA[i] = -1; s.eB[i] = -1; s.eC[i] = -1; s.pst[i] = -1; }
  for (int B = 0; blocks[B][0] >= 0; ++B) {
    int u0 = blocks[B][0], nu = blocks[B][1], c = unit_chunk(u0);
    s.blk_u0[G] = (short)u0; s.blk_nu[G] = (short)nu;
    for (int k = 0; k < nu; ++k) blk_of_unit[u0 + k] = B;
    if (c < 3) {
      for (int t = 0; t < chunk_nt(c); ++t)
        for (int k = 0; k < nu; ++k)
          for (int kl = 0; kl < unit_nk(u0); ++kl)
            for (int m = 0; m < 2; ++m) {
              s.unit[G] = (short)(u0 + k); s.ub[G] = (short)k; s.kl[G] = (short)kl; s.bi[G] = (short)(unit_k0(u0 + k) + kl);
              s.sub[G] = (short)m; s.tile[G] = (short)(chunk_t0(c) + t); s.first[G] = (short)(s.bi[G] == 0 && m == 0); ++G;
            }
    } else if (nu == 1) {
      for (int kl = 0; kl < 2; ++kl)
        for (int nf = 0; nf < 9; ++nf) {
          s.unit[G] = (short)u0; s.ub[G] = 0; s.kl[G] = (short)kl; s.bi[G] = (short)(unit_k0(u0) + kl);
          s.tile[G] = (short)nf; s.first[G] = (short)(s.bi[G] == 0); ++G;
        }
    } else {
      for (int nf = 0; nf < 9; ++nf)
        for (int k = 0; k < nu; ++k)
          for (int kl = 0; kl < 2; ++kl) {
            s.unit[G] = (short)(u0 + k); s.ub[G] = (short)k; s.kl[G] = (short)kl; s.bi[G] = (short)(unit_k0(u0 + k) + kl);
            s.tile[G] = (short)nf; ++G;
            if (nf > 0 && k == 0 && kl == 1) s.pst[G - 1] = (short)(nf - 1);
          }
    }
  }
  int store_at[NS] = {}, last_store[3] = {};
  for (int g = 0; g < NS; ++g) if (s.pst[g] >= 0) store_at[g] += 1;
  int prev = 0;
  for (int i = 0; i < 9; ++i) {
    int R = 0, D = 100000;
    for (int g = 0; g < NS1; ++g) if (s.tile[g] == i) R = g + 1;
    for (int g = R; g < NS1; ++g) if (tile_set(s.tile[g]) == tile_set(i)) { D = g; break; }
    int st = R > prev ? R : prev;
    int sp = (st + 14 < D) ? 2 : 1;
    if (st + sp * 7 >= D) s.ok = false;
    for (int j = 0; j < 8; ++j) {
      int g = st + sp * j;
      s.eA[g] = (short)(i * 8 + j); s.eB[g + 1] = (short)(i * 8 + j); s.eC[g + 2] = (short)(i * 8 + j);
      if (j == 7) { store_at[g + 2] += 1; last_store[i < 4 ? 0 : i < 7 ? 1 : 2] = g + 2; }
    }
    prev = st + sp * 8;
  }
  const int poll_at[3] = {236, 326, 388};
  for (int c = 0; c < 3; ++c) s.pub[last_store[c] + (c == 2 ? 10 : 14)] = (short)(c + 1);
  int xlast[18] = {};
  for (int g = 0; g < NS1; ++g) xlast[s.bi[g]] = g;
  int ba[96] = {}, bb[96] = {}, be[96] = {}, nb = 0;
  for (int k = 0; k < 18; ++k) for (int m = 0; m < 2; ++m) { ba[nb] = k; bb[nb] = m; be[nb] = 0; ++nb; }
  for (int c = 0; c < 3; ++c) {
    int k0 = c == 0 ? 0 : c == 1 ? 8 : 14, k1 = c == 0 ? 8 : c == 1 ? 14 : 18;
    for (int k = k0; k < k1; ++k) for (int m = 0; m < 2; ++m) {
      int e = poll_at[c] + CHK_DELAY + 1; if (xlast[k] + 1 > e) e = xlast[k] + 1;
      ba[nb] = 18 + k; bb[nb] = m; be[nb] = e; ++nb;
    }
  }
  int wa[96] = {}, wb[96] = {}, nw = 0;
  for (int U = NPRE; U < NU; ++U) for (int j = 0; j < (unit_np(U) + 7) / 8; ++j) { wa[nw] = U; wb[nw] = j; ++nw; }
  int bi = 0, wi = 0;
  while (bi < 8) { s.pro_a[s.pro_n] = (short)ba[bi]; s.pro_b[s.pro_n] = (short)bb[bi]; ++s.pro_n; ++bi; }
  int wneed[NU] = {}, bneed[36] = {};
  for (int g = NS - 1; g >= 0; --g) {
    bneed[(g >= NS1 ? 18 : 0) + s.bi[g]] = g;
    if (s.blk_u0[g] >= 0) for (int k = 0; k < s.blk_nu[g]; ++k) wneed[s.blk_u0[g] + k] = g - PF;
  }
  {
    int bars_done = 0, ta_free = 0;
    for (int g = 0; g < NS; ++g) {
      int gn = g + PF;
      if (gn < NS && s.blk_u0[gn] >= 0) bars_done = blk_of_unit[s.blk_u0[gn]];
      bool polled = false;
      for (int c = 0; c < 3; ++c) if (poll_at[c] == g) { s.op_t[g] = OP_POLL; s.op_a[g] = (short)c; s.chk[g + CHK_DELAY] = (short)(c + 1); polled = true; }
      if (polled || ta_free > g * 10 + 5) continue;
      const int base = ta_free > g * 10 - 5 ? ta_free : g * 10 - 5;
      bool wready = wi < nw && !(wa[wi] - NBUF >= 0 && blk_of_unit[wa[wi] - NBUF] > bars_done - 2);
      bool bready = bi < nb && g >= be[bi];
      int bdl = bready ? bneed[ba[bi]] : 1000000;
      int wdl = wready ? wneed[wa[wi]] - 12 : 1000000;
      if (bready && bdl <= wdl) {
        s.op_t[g] = OP_B; s.op_a[g] = (short)ba[bi]; s.op_b[g] = (short)bb[bi];
        ta_free = base + 8 * TA_COST / 10;
        if (bneed[ba[bi]] - g < s.min_lead_b) s.min_lead_b = (short)(bneed[ba[bi]] - g);
        ++bi;
      } else if (wready) {
        s.op_t[g] = OP_W; s.op_a[g] = (short)wa[wi]; s.op_b[g] = (short)wb[wi];
        const int c = unit_chunk(wa[wi]);
        int pieces = unit_np(wa[wi]) - 8 * wb[wi]; if (pieces > 8) pieces = 8;
        ta_free = base + (c == 0 ? 8 : c == 3 ? 11 : c == 1 ? 14 : 20) * TA_COST * pieces / 80;
        if (wneed[wa[wi]] - g < s.min_lead_w) s.min_lead_w = (short)(wneed[wa[wi]] - g);
        ++wi;
      }
    }
    if (bi < nb || wi < nw || s.min_lead_b <= 2 || s.min_lead_w <= 2) s.ok = false;
  }
  {
    int n = 0, wpos[NU] = {}, bpos[36] = {}, pollpos = 0, storepos = 0;
    bool bseen[36] = {};
    for (int i = 0; i < s.pro_n; ++i) { ++n; bpos[s.pro_a[i]] = n; }
    s.bar_wait[0] = (short)n;
    for (int g = 0; g < NS; ++g) {
      int gn = g + PF;
      if (gn < NS && s.blk_u0[gn] >= 0) {
        int need = 0;
        for (int k = 0; k < s.blk_nu[gn]; ++k) if (wpos[s.blk_u0[gn] + k] > need) need = wpos[s.blk_u0[gn] + k];
        s.bar_wait[gn] = (short)(n - need);
      }
      if (s.op_t[g] == OP_POLL) { ++n; pollpos = n; }
      if (s.op_t[g] == OP_B) { ++n; bpos[s.op_a[g]] = n; }
      if (s.op_t[g] == OP_W) { if ((s.op_b[g] + 1) * 8 <= unit_np(s.op_a[g])) ++n; wpos[s.op_a[g]] = n; }
      if (s.chk[g]) s.chk_wait[g] = (short)(n - pollpos);
      int bk = (g >= NS1 ? 18 : 0) + s.bi[g];
      if (!bseen[bk]) { bseen[bk] = true; s.bwait[g] = (short)(n - bpos[bk]); }
      if (store_at[g]) { n += store_at[g]; storepos = n; }
      if (s.pub[g]) { s.pub_wait[g] = (short)(n - storepos); ++n; }
    }
  }
  for (int g = 0; g < NS; ++g) if (s.bar_wait[g] > 63 || s.bwait[g] > 63 || s.pub_wait[g] > 63 || s.chk_wait[g] > 63) s.ok = false;
  return s;
}
constexpr Sched SC = make_sched();
static_assert(SC.ok, "schedule");
}

namespace p1 {
constexpr int XS = 0;
constexpr int MODS = 92160;
constexpr int QP = 456;
constexpr int OS = 40960;
constexpr int OP = 160;
}
namespace p2 {
constexpr int OSM = 0;
constexpr int RED = 36864;
constexpr int RST = RED + 32 * 37 * 8;
constexpr int X1S = mlp::RING_BYTES;
constexpr int MV = X1S + 9216;
}
constexpr int LDS_BYTES = p2::MV + 4 * 144 * 4;

DI void mem_fence() { asm volatile("" ::: "memory"); }

template <int NN, int M0>
DI void p1_gemm(char* smem, bf16x8 (&wf)[2][18], const bf16x8* wp, f32x4 (&acc)[2][5], int lane) {
  const int g = lane >> 4, m16 = lane & 15;
  const char* xs0 = smem + p1::XS + (g * 16 + (m16 ^ (2 * g))) * 16;
  const char* xs1 = smem + p1::XS + (g * 16 + (m16 ^ (2 * g + 1))) * 16;
  static_for<18>([&](auto kc) LAMBDA_INLINE {
    constexpr int k = decltype(kc)::value;
    if constexpr (k + 8 < 18) {
#pragma unroll
      for (int n = 0; n < NN; ++n) wf[n][k + 8] = wp[(n * 18 + k + 8) * 64];
    }
    bf16x8 bf[5];
#pragma unroll
    for (int m = M0; m < 5; ++m) bf[m] = *(const bf16x8*)(((k & 1) ? xs1 : xs0) + (k * 5 + m) * 1024);
#pragma unroll
    for (int n = 0; n < NN; ++n)
#pragma unroll
      for (int m = M0; m < 5; ++m) acc[n][m] = __builtin_amdgcn_mfma_f32_16x16x32_bf16(wf[n][k], bf[m], acc[n][m], 0, 0, 0);
    if constexpr (k + 8 < 18) mem_fence();
  });
}

extern "C" __global__ __launch_bounds__(512, 2) void k_main(const u16* __restrict__ x, const u16* __restrict__ w1, const u16* __restrict__ w3,
                                                 const u16* __restrict__ w2, char* __restrict__ ws, u16* __restrict__ out) {
  using namespace mlp;
  extern __shared__ __attribute__((aligned(16))) char smem[];
  const int tid = threadIdx.x, lane = tid & 63;
  const int wave = __builtin_amdgcn_readfirstlane(tid >> 6);
  const int b = blockIdx.x, xcd = b & 7, idx = b >> 3;
  const int mt = idx >> 2, ftl = idx & 3;
  const unsigned lds0 = (unsigned)(uintptr_t)(LAS char*)smem;
  const float* modp = (const float*)(ws + WS_MODP);
  unsigned* flags = (unsigned*)(ws + WS_FLAGS);
  STAMP_AT(0);
  const int a0 = ftl == 0 ? 0 : ftl == 1 ? 3 : ftl == 2 ? 2 : 5;
  const int c1 = ftl == 0 ? 4 : ftl == 1 ? 0 : ftl == 2 ? 6 : 0;
  const int e0 = ftl == 0 ? 7 : ftl == 1 ? 7 : ftl == 2 ? 0 : 3;
  const int fbase = (xcd * 4 + ftl) * 144;

  const char* sp[3];
  unsigned so[3];
  auto stage_setup = [&](auto cc_) LAMBDA_INLINE {
    constexpr int c = decltype(cc_)::value;
    const int lane = lane_now();
#pragma unroll
    for (int j = 0; j < (c == 2 ? 2 : 3); ++j) {
      int i = wave + 8 * j;
      if (c == 2) { if (i > 11) i = 11; } else { if (i > 17) i = 17; }
      const int ci = i * 64 + lane;
      if constexpr (c == 0) {
        int row = ci / 18, cc = ci % 18; if (cc > 15) cc = 15;
        sp[j] = (const char*)((cc >= 8 ? w3 : w1) + (size_t)row * F_ + fbase + a0 * 16 + (cc & 7) * 8);
      } else if constexpr (c == 1) {
        int row = ci / 12, cc = ci % 12;
        sp[j] = (const char*)((cc >= 6 ? w3 : w1) + (size_t)row * F_ + fbase + c1 * 16 + (cc >= 6 ? cc - 6 : cc) * 8);
      } else if constexpr (c == 2) {
        int row = ci >> 3, cc = ci & 7, pos = (cc >> 1) ^ ((row >> 1) & 1);
        sp[j] = (const char*)((pos >= 2 ? w3 : w1) + (size_t)row * F_ + fbase + e0 * 16 + (pos & 1) * 16 + (cc & 1) * 8);
      } else {
        int ip = i >= 9 ? i - 9 : i, c2 = ip * 64 + lane, row = c2 / 18, cc = c2 % 18;
        so[j] = (unsigned)(row * D_ + cc * 8) * 2u;
      }
    }
  };
  const u16* w2b = w2 + (size_t)xcd * 576 * D_ + ftl * 144;
  auto stage_round = [&](auto Uc, auto jc) LAMBDA_INLINE {
    constexpr int U = decltype(Uc)::value, j = decltype(jc)::value, c = unit_chunk(U);
    if constexpr (j == 0 && unit_k0(U) == 0) stage_setup(IC(c));
    const unsigned dst = lds0 + slot_of(U) * SLOT + (wave + 8 * j) * 1024;
    constexpr bool all = (j + 1) * 8 <= unit_np(U);
    if (all || wave + 8 * j < unit_np(U)) {
      if constexpr (c < 3) {
        glds16v(sp[j], dst);
      } else {
        constexpr int pA = 2 * unit_k0(U), pB = pA + 2;
        const bool h1 = wave + 8 * j >= 9;
        const int r0 = h1 ? pos_row(pB) : pos_row(pA);
        constexpr int dA = pos_row(pA + 1) - pos_row(pA) - 16, dB = pos_row(pB + 1) - pos_row(pB) - 16;
        unsigned vo = so[j];
        if constexpr (dA != 0 || dB != 0) vo += (so[j] >= 16u * D_ * 2u) ? (unsigned)((h1 ? dB : dA) * D_ * 2) : 0u;
        glds16(vo, w2b + (size_t)r0 * D_, dst);
      }
    }
    if constexpr (c < 3) sp[j] += (size_t)unit_nk(U) * 32 * F_ * 2;
  };

  const int g = lane >> 4, m16 = lane & 15;
  bf16x8 wf[2][18];
  {
    using namespace p1;
    const int rt = idx, hg = xcd;
    const float2* stat1 = (const float2*)(ws + WS_STAT1);
    const int r0 = rt * 64 - 16;
    f32x4 mp[3];
    int mo = tid * 4;
    if (tid >= 288) { const int t = tid - 288; mo = (2 + t / 36) * D_ + ftl * 144 + (t % 36) * 4; }
    if (tid < 432) {
#pragma unroll
      for (int i = 0; i < 3; ++i) mp[i] = *(const f32x4*)(modp + i * 3456 + mo);
    }
    const int r8 = lane >> 3, ch = lane & 7;
    u32x4 xv[12];
    float2 xs[12];
#pragma unroll
    for (int i = 0; i < 12; ++i) {
      const int rg = i < 10 ? i : i == 10 ? wave : 8 + (wave & 1), ln = i < 10 ? wave : 8;
      int row = r0 + rg * 8 + r8; if (row < 0) row = 0;
      xv[i] = *(const u32x4*)(x + (size_t)row * D_ + ln * 64 + ch * 8);
      xs[i] = stat1[row];
    }
    mem_fence();
    const int nf0 = wave < 4 ? 4 + 2 * wave : wave < 6 ? 2 * (wave - 4) : 6 + wave;
    const bf16x8* wp = (const bf16x8*)(ws + WS_WQF) + (size_t)(hg * 14 + nf0) * 18 * 64 + lane;
#pragma unroll
    for (int k = 0; k < 4; ++k) { wf[0][k] = wp[k * 64]; if (wave < 6) wf[1][k] = wp[(18 + k) * 64]; }
    float* mods = (float*)(smem + MODS);
    if (tid < 432) {
      f32x4 v = mp[0] + mp[1] + mp[2];
      const bool plus1 = (tid >= 144 && tid < 288) || (tid >= 360 && tid < 396);
#pragma unroll
      for (int j = 0; j < 4; ++j) { v[j] = rbf(v[j]); if (plus1) v[j] = rbf(1.f + v[j]); }
      if (tid < 288) *(f32x4*)(mods + tid * 4) = v;
      else *(f32x4*)(smem + p2::MV + (tid - 288) * 16) = v;
    }
    lds_barrier();
    auto norm8 = [&](u32x4 v, float2 st, const f32x4 (&sa)[2], const f32x4 (&aa)[2]) LAMBDA_INLINE {
      u32x4 o;
#pragma unroll
      for (int j = 0; j < 4; ++j) {
        const float lo = bflo(v[j]) * st.x + st.y, hi = bfhi(v[j]) * st.x + st.y;
        o[j] = pack_bf16(lo * aa[j >> 1][(2 * j) & 3] + sa[j >> 1][(2 * j) & 3], hi * aa[j >> 1][(2 * j + 1) & 3] + sa[j >> 1][(2 * j + 1) & 3]);
      }
      return o;
    };
    {
      const int gq = ch & 3, kb = ch >> 2, swz = 2 * gq + kb;
#pragma unroll
      for (int part = 0; part < 2; ++part) {
        const int ln = part ? 8 : wave;
        f32x4 sa[2], aa[2];
        const float* mq = mods + ln * 64 + ch * 8;
        sa[0] = *(const f32x4*)mq; sa[1] = *(const f32x4*)(mq + 4);
        aa[0] = *(const f32x4*)(mq + 576); aa[1] = *(const f32x4*)(mq + 580);
        char* dst = smem + XS + (2 * ln + kb) * 5 * 1024 + gq * 256;
#pragma unroll
        for (int i = part ? 10 : 0; i < (part ? 12 : 10); ++i) {
          const int rg = i < 10 ? i : i == 10 ? wave : 8 + (wave & 1);
          const u32x4 o = norm8(xv[i], xs[i], sa, aa);
          if (i < 11 || wave < 2) *(u32x4*)(dst + (rg >> 1) * 1024 + (((rg & 1) * 8 + r8) ^ swz) * 16) = o;
          if (i >= 1 && i < 9 && (i & 1)) {
            constexpr int dummy = 0; (void)dummy;
            const int k = 4 + (i >> 1);
            wf[0][k] = wp[k * 64]; if (wave < 6) wf[1][k] = wp[(18 + k) * 64];
            mem_fence();
          }
        }
      }
    }
    unsigned sink = 0;
    lds_barrier();
    STAMP_AT(1);
    f32x4 acc[2][5] = {};
    if (wave < 4) p1_gemm<2, 0>(smem, wf, wp, acc, lane);
    else if (wave < 6) p1_gemm<2, 1>(smem, wf, wp, acc, lane);
    else {
      p1_gemm<1, 0>(smem, wf, wp, acc, lane);
      const int l54 = lane < 54 ? lane : 53, r3 = l54 / 18, cc = l54 % 18;
      const unsigned o13 = (unsigned)(r3 * F_ + cc * 32) * 2u, o2 = (unsigned)(r3 * D_ + cc * 32) * 2u;
      const int rr = idx * 18 + (wave - 6) * 9;
#pragma unroll
      for (int i = 0; i < 3; ++i) {
        asm volatile("global_load_dword %0, %1, %2" : "+v"(sink) : "v"(o13), "s"(w1 + (size_t)(rr + 3 * i) * F_ + xcd * 576) : "memory");
        asm volatile("global_load_dword %0, %1, %2" : "+v"(sink) : "v"(o13), "s"(w3 + (size_t)(rr + 3 * i) * F_ + xcd * 576) : "memory");
        asm volatile("global_load_dword %0, %1, %2" : "+v"(sink) : "v"(o2), "s"(w2 + (size_t)(xcd * 576 + rr + 3 * i) * D_) : "memory");
      }
    }
    lds_barrier();
    STAMP_AT(2);
#pragma unroll
    for (int n = 0; n < 2; ++n)
#pragma unroll
      for (int m = 0; m < 5; ++m) {
        if (n == 1 && wave >= 6) continue;
        u32x2 v = {pack_bf16(acc[n][m][0], acc[n][m][1]), pack_bf16(acc[n][m][2], acc[n][m][3])};
        *(u32x2*)(smem + (m * 16 + m16) * QP + ((nf0 + n) * 16 + 4 * g) * 2) = v;
      }
    lds_barrier();
    {
      const bf16x8* wo0 = (const bf16x8*)(ws + WS_WOF) + (size_t)(ftl * 9 + wave) * 18 * 64 + lane;
      const bf16x8* wo8 = (const bf16x8*)(ws + WS_WOF) + (size_t)(ftl * 9 + 8) * 18 * 64 + lane;
      auto wo_load = [&](int k) LAMBDA_INLINE { wf[0][k] = wo0[k * 64]; if (wave < 2) wf[1][k] = wo8[k * 64]; };
      typedef __bf16 bf2 __attribute__((ext_vector_type(2)));
      const int rl = (wave >> 1) * 16 + m16, hd = (wave & 1) * 2 + (g & 1), hw = lane >> 5;
      const int grow = rt * 64 + rl;
      const unsigned* qrow = (const unsigned*)(smem + (rl + 16) * QP) + hd * 9;
      unsigned qv[9];
#pragma unroll
      for (int d = 0; d < 9; ++d) qv[d] = qrow[d];
      wo_load(0); wo_load(1);
      float lg[4];
#pragma unroll
      for (int w = 0; w < 4; ++w) {
        const int ww = hw * 4 + w;
        const unsigned* kr = (const unsigned*)(smem + (rl + 16 - 2 * ww) * QP) + 36 + hd * 9;
        float a = 0.f;
#pragma unroll
        for (int d = 0; d < 9; ++d) a = __builtin_amdgcn_fdot2_f32_bf16(__builtin_bit_cast(bf2, qv[d]), __builtin_bit_cast(bf2, kr[d]), a, false);
        a *= 0.23570226039551584f;
        lg[w] = (grow - 2 * ww >= 0) ? a : -1e9f;
        wo_load(2 + 2 * w); wo_load(3 + 2 * w);
        mem_fence();
      }
      float mxv = fmaxf(fmaxf(lg[0], lg[1]), fmaxf(lg[2], lg[3]));
      mxv = fmaxf(mxv, __shfl_xor(mxv, 32));
      float pw[8], sum = 0.f;
#pragma unroll
      for (int w = 0; w < 4; ++w) { lg[w] = __builtin_amdgcn_exp2f((lg[w] - mxv) * 1.44269504089f); sum += lg[w]; }
      sum += __shfl_xor(sum, 32);
      const float inv = __builtin_amdgcn_rcpf(sum);
#pragma unroll
      for (int w = 0; w < 4; ++w) {
        const float mine = lg[w] * inv, other = __shfl_xor(mine, 32);
        pw[w] = hw ? other : mine; pw[4 + w] = hw ? mine : other;
      }
      float o[10] = {};
#pragma unroll
      for (int w = 0; w < 8; ++w) {
        const unsigned* vr = (const unsigned*)(smem + (rl + 16 - 2 * w) * QP) + 72 + hd * 9 + hw * 5;
#pragma unroll
        for (int d = 0; d < 5; ++d) { const unsigned v = vr[d]; o[2 * d] += pw[w] * bflo(v); o[2 * d + 1] += pw[w] * bfhi(v); }
        wo_load(10 + w);
        if (w & 1) mem_fence();
      }
      unsigned* orow = (unsigned*)(smem + OS + rl * OP) + hd * 9 + hw * 5;
#pragma unroll
      for (int d = 0; d < 5; ++d) if (d < 4 || hw == 0) orow[d] = pack_bf16(o[2 * d], o[2 * d + 1]);
    }
    lds_barrier();
    STAMP_AT(3);
    const rsrc_t ors = mk_rsrc(ws + WS_OF, S_ * D_ * 2);
#pragma unroll
    for (int it = 0; it < 2; ++it) {
      const int pc = tid + it * 512;
      if (pc < 576) {
        const int row = pc & 63, c = pc >> 6;
        const u32x4 v = *(const u32x4*)(smem + OS + row * OP + c * 16);
        const int c8 = hg * 9 + c, mf = rt * 4 + (row >> 4);
        st16_sc1(ors, (unsigned)(((mf * 18 + (c8 >> 2)) * 64 + (c8 & 3) * 16 + (row & 15)) * 16), 0, v);
      }
    }
    asm volatile("s_waitcnt vmcnt(0)" : "+v"(sink)::"memory");
    lds_barrier();
    if (tid == 0) set_flag(flags + FL_O + rt * 16 + hg, 1u);
    STAMP_AT(4);
  }
  static_for<15>([&](auto ic) LAMBDA_INLINE { stage_round(IC(decltype(ic)::value / 3), IC(decltype(ic)::value % 3)); });

  {
    using namespace p2;
    const int rt = mt * 8 + xcd, nt = ftl;
    u32x2 xr[3];
#pragma unroll
    for (int t = 0; t < 3; ++t) {
      const int nf = t < 2 ? wave : 8, mi = t < 2 ? t : (wave & 1);
      xr[t] = *(const u32x2*)(x + (size_t)(rt * 32 + mi * 16 + m16) * D_ + nt * 144 + nf * 16 + 4 * g);
    }
    wait_flags(flags + FL_O + (rt >> 1) * 16, 8, 1u, lane);
    STAMP_AT(5);
    const rsrc_t ors = mk_rsrc(ws + WS_OF, S_ * D_ * 2);
    u32x4 ov[5];
#pragma unroll
    for (int i = 0; i < 5; ++i) {
      const int f = wave + 8 * i;
      if (f < 36) ov[i] = ld16_sc1(ors, lane * 16, (unsigned)((rt * 36 + f) * 1024));
    }
#pragma unroll
    for (int i = 0; i < 5; ++i) {
      const int f = wave + 8 * i;
      if (f < 36) *(u32x4*)(smem + OSM + f * 1024 + lane * 16) = ov[i];
    }
    lds_barrier();
    STAMP_AT(6);
    f32x4 acc[3] = {};
#pragma unroll
    for (int k = 0; k < 18; ++k) {
      const bf16x8 b0 = *(const bf16x8*)(smem + OSM + k * 1024 + lane * 16), b1 = *(const bf16x8*)(smem + OSM + (18 + k) * 1024 + lane * 16);
      acc[0] = __builtin_amdgcn_mfma_f32_16x16x32_bf16(wf[0][k], b0, acc[0], 0, 0, 0);
      acc[1] = __builtin_amdgcn_mfma_f32_16x16x32_bf16(wf[0][k], b1, acc[1], 0, 0, 0);
      if (wave < 2) acc[2] = __builtin_amdgcn_mfma_f32_16x16x32_bf16(wf[1][k], wave ? b1 : b0, acc[2], 0, 0, 0);
    }
    float2* red = (float2*)(smem + RED);
    const float* mv = (const float*)(smem + MV);
    f32x4 xv[3];
#pragma unroll
    for (int t = 0; t < 3; ++t) {
      if (t == 2 && wave >= 2) continue;
      const int nf = t < 2 ? wave : 8, mi = t < 2 ? t : (wave & 1);
      const f32x4 gv = *(const f32x4*)(mv + nf * 16 + 4 * g);
      u32x2 xb = {pack_bf16(bflo(xr[t][0]) + gv[0] * acc[t][0], bfhi(xr[t][0]) + gv[1] * acc[t][1]),
                  pack_bf16(bflo(xr[t][1]) + gv[2] * acc[t][2], bfhi(xr[t][1]) + gv[3] * acc[t][3])};
      *(u32x2*)(smem + X1S + ((nf * 2 + mi) * 64 + lane) * 8) = xb;
      f32x4 v = {bflo(xb[0]), bfhi(xb[0]), bflo(xb[1]), bfhi(xb[1])};
      xv[t] = v;
      const float mu = (v[0] + v[1] + v[2] + v[3]) * 0.25f;
      const float q = (v[0] - mu) * (v[0] - mu) + (v[1] - mu) * (v[1] - mu) + (v[2] - mu) * (v[2] - mu) + (v[3] - mu) * (v[3] - mu);
      red[(mi * 16 + m16) * 37 + nf * 4 + g] = make_float2(mu, q);
    }
    lds_barrier();
    STAMP_AT(7);
    if (wave == 0) {
      GAS u64* stg = (GAS u64*)(flags + FL_ST) + (size_t)rt * 128;
      const int row = lane & 31, hf = lane >> 5;
      float2 e[18];
#pragma unroll
      for (int i = 0; i < 18; ++i) e[i] = red[row * 37 + hf * 18 + i];
      float mu = 0.f;
#pragma unroll
      for (int i = 0; i < 18; ++i) mu += e[i].x;
      mu += __shfl_xor(mu, 32);
      mu *= (1.f / 36.f);
      float q = 0.f;
#pragma unroll
      for (int i = 0; i < 18; ++i) q += e[i].y + 4.f * (e[i].x - mu) * (e[i].x - mu);
      q += __shfl_xor(q, 32);
      if (lane < 32) __hip_atomic_store(stg + nt * 32 + row, ((u64)(__float_as_uint(q) | 0x80000000u) << 32) | __float_as_uint(mu), RLX_AGENT);
      u64 e0v = 0, e1v = 0;
      for (unsigned spin = 0; spin < SPIN_CAP; ++spin) {
        e0v = __hip_atomic_load(stg + lane, RLX_AGENT);
        e1v = __hip_atomic_load(stg + 64 + lane, RLX_AGENT);
        if (__all((e0v >> 63) && (e1v >> 63))) break;
        __builtin_amdgcn_s_sleep(1);
      }
      const float m0 = __uint_as_float((unsigned)e0v), m1 = __uint_as_float((unsigned)e1v);
      const float q0 = __uint_as_float((unsigned)(e0v >> 32) & 0x7fffffffu), q1 = __uint_as_float((unsigned)(e1v >> 32) & 0x7fffffffu);
      float ms = m0 + m1; ms += __shfl_xor(ms, 32);
      const float mean = 0.25f * ms;
      float qs = q0 + q1 + 144.f * ((m0 - mean) * (m0 - mean) + (m1 - mean) * (m1 - mean));
      qs += __shfl_xor(qs, 32);
      const float rstd = __builtin_amdgcn_rsqf(qs * (1.f / D_) + 1e-5f);
      if (lane < 32) ((float2*)(smem + RST))[row] = make_float2(rstd, -mean * rstd);
    }
    lds_barrier();
    STAMP_AT(8);
    const rsrc_t hrs2 = mk_rsrc(ws + WS_H2F, S_ * D_ * 2);
#pragma unroll
    for (int t = 0; t < 3; ++t) {
      if (t == 2 && wave >= 2) continue;
      const int nf = t < 2 ? wave : 8, mi = t < 2 ? t : (wave & 1);
      const float2 st = ((const float2*)(smem + RST))[mi * 16 + m16];
      const f32x4 sbv = *(const f32x4*)(mv + 144 + nf * 16 + 4 * g), cbv = *(const f32x4*)(mv + 288 + nf * 16 + 4 * g);
      float h[4];
#pragma unroll
      for (int j = 0; j < 4; ++j) h[j] = (xv[t][j] * st.x + st.y) * cbv[j] + sbv[j];
      const int nfg = nt * 9 + nf;
      u32x2 o = {pack_bf16(h[0], h[1]), pack_bf16(h[2], h[3])};
      st8_sc1(hrs2, (unsigned)((g * 32 + mi * 16 + m16) * 16), (unsigned)((rt * 18 + (nfg >> 1)) * 2048 + (nfg & 1) * 8), o);
    }
    wait_vm<0>();
    lds_barrier();
    if (tid == 0) set_flag(flags + FL_H2 + rt * 16 + nt, 1u);
    STAMP_AT(9);
  }
  static_for<9>([&](auto ic) LAMBDA_INLINE { stage_round(IC(5 + decltype(ic)::value / 3), IC(decltype(ic)::value % 3)); });

  bf16x8 xf[18][2];
  const int mf0 = (mt * 8 + wave) * 2;
  const char* xb = ws + WS_H2F + (size_t)(mt * 8 + wave) * 36 * 1024;
  const char* hb = ws + WS_HF + ((size_t)mf0 * 8 + xcd) * 18432;
  const unsigned* hfl = flags + FL_H + ((xcd * 8 + mt) * 8 + wave) * 16;
  const unsigned l16 = lane * 16;
  unsigned flagv;
  auto bload = [&](auto ac, auto mc) LAMBDA_INLINE {
    constexpr int a = decltype(ac)::value, mi = decltype(mc)::value;
    if constexpr (a < 18) {
      constexpr int o = (a * 2 + mi) * 1024;
      gload16_sc1<o % 4096>(xf[a][mi], l16, xb + (o / 4096) * 4096);
    } else {
      constexpr int o = (a - 18) * 1024;
      gload16_sc1<o % 4096>(xf[a - 18][mi], l16, hb + mi * 8 * 18432 + (o / 4096) * 4096);
    }
  };
  wait_flags(flags + FL_H2 + (mt * 8 + wave) * 16, 4, 1u, lane);
  STAMP_AT(10);
  static_for<SC.pro_n>([&](auto ic) LAMBDA_INLINE { constexpr int i = decltype(ic)::value; bload(IC(SC.pro_a[i]), IC(SC.pro_b[i])); });

  unsigned ubase[3][2];
  auto block_barrier = [&](auto Gc) LAMBDA_INLINE {
    constexpr int G = decltype(Gc)::value, u0 = SC.blk_u0[G], nu = SC.blk_nu[G], c = unit_chunk(u0);
    sched_fence();
    wait_vm<SC.bar_wait[G]>();
    barrier_raw();
    const int ln = lane_now(), p = ln & 3, q = (ln >> 2) & 3, blk = (ln >> 4) & 1, hh = ln >> 5;
#pragma unroll
    for (int k = 0; k < nu; ++k) {
      const unsigned sb = lds0 + slot_of(u0 + k) * SLOT + 8 * p;
      if constexpr (c == 0) ubase[k][0] = sb + (4 * hh + q) * 288 + blk * 128;
      else if constexpr (c == 1) ubase[k][0] = sb + (4 * hh + q) * 192 + blk * 96;
      else if constexpr (c == 2) {
        ubase[k][0] = sb + (4 * hh + q) * 128 + blk * 64 + 32 * (q >> 1);
        ubase[k][1] = sb + (4 * hh + q) * 128 + blk * 64 + 32 * (1 - (q >> 1));
      } else ubase[k][0] = sb + (16 * hh + 4 * blk + q) * 288;
      asm volatile("" : "+v"(ubase[k][0]));
      if constexpr (c == 2) asm volatile("" : "+v"(ubase[k][1]));
    }
    sched_fence();
  };
  auto afrag = [&](auto Gc) LAMBDA_INLINE {
    constexpr int G = decltype(Gc)::value, U = SC.unit[G], c = unit_chunk(U), k = SC.ub[G], kl = SC.kl[G], sb = SC.sub[G];
    if constexpr (c == 0) {
      constexpr unsigned o = (kl * 32 + 8 * sb) * 288 + SC.tile[G] * 32;
      return tr_frag(ubase[k][0] + o, ubase[k][0] + o + 16 * 288);
    } else if constexpr (c == 1) {
      constexpr unsigned o = (kl * 32 + 8 * sb) * 192 + (SC.tile[G] - 4) * 32;
      return tr_frag(ubase[k][0] + o, ubase[k][0] + o + 16 * 192);
    } else if constexpr (c == 2) {
      constexpr unsigned o = (kl * 32 + 8 * sb) * 128;
      return tr_frag(ubase[k][SC.tile[G] - 7] + o, ubase[k][SC.tile[G] - 7] + o + 16 * 128);
    } else {
      constexpr unsigned o = kl * 32 * 288 + SC.tile[G] * 32;
      return tr_frag(ubase[k][0] + o, ubase[k][0] + o + 8 * 288);
    }
  };
  f32x16 acc[4];
  f32x4 acc2[9][2];
  bf16x8 af[PF + 1];
  float ta[2], tp[2], hv[2];
  u32x4 pk;
  const rsrc_t hrs = mk_rsrc(ws + WS_HF, 128 * 8 * 18432);
  const rsrc_t prs = mk_rsrc(ws + WS_PART, 64 * 32 * 9 * 1024);
  const unsigned hso = (unsigned)((mf0 * 8 + xcd) * 18432);
  const unsigned tile = (unsigned)(mt * 4 + ftl);
  auto epi = [&](auto Gc) LAMBDA_INLINE {
    constexpr int G = decltype(Gc)::value;
    if constexpr (SC.eC[G] >= 0) {
      constexpr int e = SC.eC[G], i = e >> 3, j = e & 7;
      hv[j & 1] = tp[e & 1] * ta[e & 1];
      if constexpr (j & 1) {
        pk[j >> 1] = pack_bf16(hv[0], hv[1]);
        asm volatile("" : "+v"(pk));
      } else {
        asm volatile("" : "+v"(hv[0]));
      }
      if constexpr (j == 7) {
        const int ln = lane_now();
        const int pos = i < 4 ? 4 * ftl + i : i < 7 ? 16 + 3 * ftl + (i - 4) : 28 + 2 * ftl + (i - 7);
        st16_sc1(hrs, (unsigned)(((ln >> 4) & 1) * 8 * 18432 + (ln >> 5) * 256 + (ln & 15) * 16), hso + pos * 512, pk);
      }
    }
    if constexpr (SC.eB[G] >= 0) {
      constexpr int e = SC.eB[G];
      ta[e & 1] = __builtin_amdgcn_rcpf(1.f + ta[e & 1]);
      asm volatile("" : "+v"(ta[e & 1]));
    }
    if constexpr (SC.eA[G] >= 0) {
      constexpr int e = SC.eA[G], i = e >> 3, j = e & 7, st = tile_set(i);
      const float gv = acc[st][j];
      tp[e & 1] = gv * acc[st][8 + j];
      ta[e & 1] = __builtin_amdgcn_exp2f(gv * -1.44269504089f);
      asm volatile("" : "+v"(ta[e & 1]), "+v"(tp[e & 1]));
    }
  };
  auto part_store = [&](auto nc) LAMBDA_INLINE {
    constexpr int nf = decltype(nc)::value;
    u32x4 v = {pack_bf16(acc2[nf][0][0], acc2[nf][0][1]), pack_bf16(acc2[nf][0][2], acc2[nf][0][3]),
               pack_bf16(acc2[nf][1][0], acc2[nf][1][1]), pack_bf16(acc2[nf][1][2], acc2[nf][1][3])};
    st16_sc1(prs, l16, (((wave * 8 + xcd) * 32 + tile) * 9 + nf) * 1024, v);
  };

  block_barrier(IC(0));
  static_for<PF>([&](auto ic) LAMBDA_INLINE { constexpr int G = decltype(ic)::value; af[G % (PF + 1)] = afrag(IC(G)); });
  static_for<NS>([&](auto ic) LAMBDA_INLINE {
    constexpr int G = decltype(ic)::value, GN = G + PF, kk = SC.bi[G];
    if constexpr (G == NS1) STAMP_AT(11);
    if constexpr (G == 1) STAMP_AT(16);
    if constexpr (G == 48) STAMP_AT(17);
    if constexpr (G == 112) STAMP_AT(18);
    if constexpr (G == 144) STAMP_AT(19);
    if constexpr (G == 198) STAMP_AT(20);
    if constexpr (G == 252) STAMP_AT(21);
    if constexpr (G == 396) STAMP_AT(22);
    if constexpr (G == 450) STAMP_AT(23);
    if constexpr (GN < NS) {
      if constexpr (SC.blk_u0[GN] >= 0) block_barrier(IC(GN));
    }
    if constexpr (SC.op_t[G] == OP_W) stage_round(IC(SC.op_a[G]), IC(SC.op_b[G]));
    if constexpr (SC.op_t[G] == OP_B) bload(IC(SC.op_a[G]), IC(SC.op_b[G]));
    if constexpr (SC.op_t[G] == OP_POLL) gload4_sc1(flagv, (lane_now() & 3) * 4, hfl);
    if constexpr (SC.chk[G] != 0) {
      asm volatile("s_waitcnt vmcnt(%1)" : "+v"(flagv) : "i"(SC.chk_wait[G]) : "memory");
      if (!__all(flagv >= (unsigned)SC.chk[G])) wait_flags(hfl, 4, (unsigned)SC.chk[G], lane_now());
    }
    if constexpr (SC.bwait[G] >= 0)
      asm volatile("s_waitcnt vmcnt(%2)" : "+v"(xf[kk][0]), "+v"(xf[kk][1]) : "i"(SC.bwait[G]) : "memory");
    if constexpr (G < NS1) {
      constexpr int st = tile_set(SC.tile[G]);
      const f32x16 z = {};
      acc[st] = __builtin_amdgcn_mfma_f32_32x32x16_bf16(af[G % (PF + 1)], xf[kk][SC.sub[G]], SC.first[G] ? z : acc[st], 0, 0, 0);
      af[GN % (PF + 1)] = afrag(IC(GN));
    } else {
      constexpr int nf = SC.tile[G];
      const f32x4 z = {};
      acc2[nf][0] = __builtin_amdgcn_mfma_f32_16x16x32_bf16(af[G % (PF + 1)], xf[kk][0], SC.first[G] ? z : acc2[nf][0], 0, 0, 0);
      if constexpr (GN < NS) af[GN % (PF + 1)] = afrag(IC(GN));
      acc2[nf][1] = __builtin_amdgcn_mfma_f32_16x16x32_bf16(af[G % (PF + 1)], xf[kk][1], SC.first[G] ? z : acc2[nf][1], 0, 0, 0);
    }
    epi(IC(G));
    if constexpr (SC.pst[G] >= 0) part_store(IC(SC.pst[G]));
    if constexpr (SC.pub[G] != 0) {
      wait_vm<SC.pub_wait[G]>();
      if (lane_now() == 0) set_flag((unsigned*)hfl + ftl, (unsigned)SC.pub[G]);
    }
    sched_fence();
  });
  STAMP_AT(12);

  {
    part_store(IC(8));
    const int ln = lane_now(), gg = ln >> 4, mm = ln & 15;
    wait_vm<0>();
    if (ln == 0) set_flag(flags + FL_P + (tile * 8 + wave) * 16 + xcd, 1u);
    STAMP_AT(13);
    wait_flags(flags + FL_P + (tile * 8 + xcd) * 16, 8, 1u, ln);
    STAMP_AT(14);
    u32x4 pv[8]; u32x2 pe[8];
#pragma unroll
    for (int s = 0; s < 8; ++s) {
      pv[s] = ld16_sc1(prs, ln * 16, (((xcd * 8 + s) * 32 + tile) * 9 + wave) * 1024);
      if (wave < 2) pe[s] = ld8_sc1(prs, ln * 16 + wave * 8, (((xcd * 8 + s) * 32 + tile) * 9 + 8) * 1024);
    }
    float y[3][4] = {};
#pragma unroll
    for (int s = 0; s < 8; ++s) {
#pragma unroll
      for (int t = 0; t < 2; ++t) {
        y[t][0] += bflo(pv[s][2 * t]); y[t][1] += bfhi(pv[s][2 * t]); y[t][2] += bflo(pv[s][2 * t + 1]); y[t][3] += bfhi(pv[s][2 * t + 1]);
      }
      if (wave < 2) { y[2][0] += bflo(pe[s][0]); y[2][1] += bfhi(pe[s][0]); y[2][2] += bflo(pe[s][1]); y[2][3] += bfhi(pe[s][1]); }
    }
#pragma unroll
    for (int t = 0; t < 3; ++t) {
      if (t == 2 && wave >= 2) continue;
      const int nf = t < 2 ? wave : 8, mi = t < 2 ? t : (wave & 1);
      const u32x2 x1 = *(const u32x2*)(smem + p2::X1S + ((nf * 2 + mi) * 64 + ln) * 8);
      const f32x4 gv = *(const f32x4*)(smem + p2::MV + (432 + nf * 16 + 4 * gg) * 4);
      u32x2 o = {pack_bf16(bflo(x1[0]) + gv[0] * y[t][0], bfhi(x1[0]) + gv[1] * y[t][1]),
                 pack_bf16(bflo(x1[1]) + gv[2] * y[t][2], bfhi(x1[1]) + gv[3] * y[t][3])};
      *(u32x2*)(out + (size_t)((mt * 8 + xcd) * 32 + mi * 16 + mm) * D_ + ftl * 144 + nf * 16 + 4 * gg) = o;
    }
  }
  STAMP_AT(15);
}

extern "C" __global__ void k_tail() {}

extern "C" void kernel_launch(void* const* d_in, const int* in_sizes, int n_in, void* d_out, int out_size, void* d_ws,
                              size_t ws_size, hipStream_t stream) {
  const u16* x = (const u16*)d_in[0];
  const u16* wqkv = (const u16*)d_in[1];
  const u16* wo = (const u16*)d_in[2];
  const u16* w1 = (const u16*)d_in[4];
  const u16* w3 = (const u16*)d_in[5];
  const u16* w2 = (const u16*)d_in[6];
  const u16* c = (const u16*)d_in[7];
  const u16* wmod = (const u16*)d_in[8];
  (void)hipFuncSetAttribute((const void*)k_main, hipFuncAttributeMaxDynamicSharedMemorySize, LDS_BYTES);
  k_pre<<<k0::NB, 256, 0, stream>>>(x, wqkv, wo, c, wmod, (char*)d_ws);
  k_main<<<256, 512, LDS_BYTES, stream>>>(x, w1, w3, w2, (char*)d_ws, (u16*)d_out);
}
```

```cpp
#include <hip/hip_runtime.h>
#include <hip/hip_bf16.h>
#include <stdint.h>
#include <utility>

typedef unsigned short u16;
typedef unsigned long long u64;
typedef short bf16x8 __attribute__((ext_vector_type(8)));
typedef short s16x4 __attribute__((ext_vector_type(4)));
typedef float f32x4 __attribute__((ext_vector_type(4)));
typedef float f32x16 __attribute__((ext_vector_type(16)));
typedef unsigned u32x4 __attribute__((ext_vector_type(4)));
typedef unsigned u32x2 __attribute__((ext_vector_type(2)));
#define LAS __attribute__((address_space(3)))
#define GAS __attribute__((address_space(1)))
#define DI __device__ __forceinline__
#define LAMBDA_INLINE __attribute__((always_inline))
#define IC(x) std::integral_constant<int, (x)>{}
#define RLX_AGENT __ATOMIC_RELAXED, __HIP_MEMORY_SCOPE_AGENT

constexpr int S_ = 2048, D_ = 576, F_ = 4608;

#define STAMP_AT(i) do {} while (0)

template <int... Is, class Fn> DI void static_for_impl(std::integer_sequence<int, Is...>, Fn&& f) { (f(std::integral_constant<int, Is>{}), ...); }
template <int N, class Fn> DI void static_for(Fn&& f) { static_for_impl(std::make_integer_sequence<int, N>{}, f); }

DI float bf2f(u16 v) { return __uint_as_float(((unsigned)v) << 16); }
DI float bflo(unsigned v) { return __uint_as_float(v << 16); }
DI float bfhi(unsigned v) { return __uint_as_float(v & 0xffff0000u); }
DI unsigned pack_bf16(float lo, float hi) {
  typedef __bf16 bf2 __attribute__((ext_vector_type(2)));
  typedef float f2 __attribute__((ext_vector_type(2)));
  f2 v = {lo, hi};
  bf2 r = __builtin_convertvector(v, bf2);
  return __builtin_bit_cast(unsigned, r);
}
DI float rbf(float f) { return __uint_as_float(pack_bf16(f, 0.f) << 16); }
DI float silu_f(float g) { return g * __builtin_amdgcn_rcpf(1.f + __builtin_amdgcn_exp2f(g * -1.44269504089f)); }

template <int N> DI void wait_vm() { asm volatile("s_waitcnt vmcnt(%0)" ::"i"(N) : "memory"); }
DI void barrier_raw() { asm volatile("" ::: "memory"); __builtin_amdgcn_s_barrier(); asm volatile("" ::: "memory"); }
DI void lds_barrier() { asm volatile("s_waitcnt lgkmcnt(0)" ::: "memory"); __builtin_amdgcn_s_barrier(); asm volatile("" ::: "memory"); }
DI void sched_fence() { __builtin_amdgcn_sched_barrier(0); }
DI int lane_now() {
  int l = __builtin_amdgcn_mbcnt_hi(~0u, __builtin_amdgcn_mbcnt_lo(~0u, 0u));
  asm volatile("" : "+v"(l));
  return l;
}

DI void glds16(unsigned voff, const void* sbase, unsigned lds_dst) {
  unsigned keep;
  asm volatile("s_mov_b32 %0, m0\n\ts_mov_b32 m0, %3\n\ts_nop 0\n\tglobal_load_lds_dwordx4 %1, %2\n\ts_mov_b32 m0, %0"
               : "=&s"(keep) : "v"(voff), "s"(sbase), "s"(lds_dst) : "memory");
}
DI void glds16v(const void* vaddr, unsigned lds_dst) {
  unsigned keep;
  asm volatile("s_mov_b32 %0, m0\n\ts_mov_b32 m0, %2\n\ts_nop 0\n\tglobal_load_lds_dwordx4 %1, off\n\ts_mov_b32 m0, %0"
               : "=&s"(keep) : "v"(vaddr), "s"(lds_dst) : "memory");
}
template <int IMM> DI void gload16_sc1(bf16x8& dst, unsigned voff, const void* sbase) {
  asm volatile("global_load_dwordx4 %0, %1, %2 offset:%3 sc1" : "=v"(dst) : "v"(voff), "s"(sbase), "i"(IMM) : "memory");
}
DI void gload8_sc1(u32x2& dst, unsigned voff, const void* sbase) {
  asm volatile("global_load_dwordx2 %0, %1, %2 sc1" : "=v"(dst) : "v"(voff), "s"(sbase) : "memory");
}
DI void gload4_sc1(unsigned& dst, unsigned voff, const void* sbase) {
  asm volatile("global_load_dword %0, %1, %2 sc1" : "=v"(dst) : "v"(voff), "s"(sbase) : "memory");
}
DI s16x4 tr_read(unsigned addr) { return __builtin_amdgcn_ds_read_tr16_b64_v4i16((LAS s16x4*)(uintptr_t)addr); }
DI bf16x8 tr_frag(unsigned a0, unsigned a1) {
  s16x4 lo = tr_read(a0), hi = tr_read(a1);
  return __builtin_shufflevector(lo, hi, 0, 1, 2, 3, 4, 5, 6, 7);
}
typedef __amdgpu_buffer_rsrc_t rsrc_t;
DI rsrc_t mk_rsrc(const void* p, unsigned bytes) { return __builtin_amdgcn_make_buffer_rsrc((void*)p, 0, bytes, 0x00020000); }
DI void st16_sc1(rsrc_t r, unsigned voff, unsigned soff, u32x4 v) {
  __builtin_amdgcn_raw_buffer_store_b128(v, r, voff, soff, 16);
  asm volatile("s_nop 1" ::"v"(v) : "memory");
}
DI void st8_sc1(rsrc_t r, unsigned voff, unsigned soff, u32x2 v) { __builtin_amdgcn_raw_buffer_store_b64(v, r, voff, soff, 16); }
DI u32x4 ld16(rsrc_t r, unsigned voff, unsigned soff) { return __builtin_amdgcn_raw_buffer_load_b128(r, voff, soff, 0); }
DI u32x2 ld8(rsrc_t r, unsigned voff, unsigned soff) { return __builtin_amdgcn_raw_buffer_load_b64(r, voff, soff, 0); }
DI bf16x8 ldfrag(rsrc_t r, unsigned voff, unsigned soff) { return __builtin_bit_cast(bf16x8, ld16(r, voff, soff)); }
DI f32x4 ldf4(rsrc_t r, unsigned voff, unsigned soff) { return __builtin_bit_cast(f32x4, ld16(r, voff, soff)); }
DI u32x4 ld16_sc1(rsrc_t r, unsigned voff, unsigned soff) { return __builtin_amdgcn_raw_buffer_load_b128(r, voff, soff, 16); }
DI u32x2 ld8_sc1(rsrc_t r, unsigned voff, unsigned soff) { return __builtin_amdgcn_raw_buffer_load_b64(r, voff, soff, 16); }

constexpr unsigned SPIN_CAP = 1u << 15;
DI void wait_flags(const unsigned* f, int n, unsigned need, int lane) {
  const GAS unsigned* p = (const GAS unsigned*)f + (lane < n ? lane : 0);
#pragma nounroll
  for (unsigned spin = 0; spin < SPIN_CAP; ++spin) {
    const unsigned v = __hip_atomic_load(p, RLX_AGENT);
    if (__all(v >= need)) break;
    __builtin_amdgcn_s_sleep(1);
  }
  asm volatile("" ::: "memory");
}
DI void set_flag(unsigned* f, unsigned v) { __hip_atomic_store((GAS unsigned*)f, v, RLX_AGENT); }

constexpr size_t WS_MODF = 0;
constexpr size_t WS_MODB = 65536;
constexpr size_t WS_N1 = 60u << 20;
constexpr size_t WS_FLAGS = 131072;
constexpr size_t WS_OF = 1u << 20;
constexpr size_t WS_H2F = 4u << 20;
constexpr size_t WS_WQF = 8u << 20;
constexpr size_t WS_WOF = 12u << 20;
constexpr size_t WS_HF = 16u << 20;
constexpr size_t WS_PART = 40u << 20;
constexpr int FL_O = 0;
constexpr int FL_H2 = 2048;
constexpr int FL_H = 4096;
constexpr int FL_P = FL_H + 8192;
constexpr int FL_ST = FL_P + 4096;
constexpr int FLAG_WORDS = FL_ST + 16384;

namespace k0 {
constexpr int B_MOD = 72, B_PO = 184, B_STAT = 208, B_CODE = 240, B_FLAG = 248, NB = 256;
}
DI void k_pre_body(const u16* __restrict__ x, const u16* __restrict__ wqkv, const u16* __restrict__ wo,
                   const u16* __restrict__ cvec, const u16* __restrict__ wmod, char* __restrict__ ws) {
  using namespace k0;
  __shared__ __attribute__((aligned(16))) char sm[8 * 32 * 160];
  const int tid = threadIdx.x, lane = tid & 63, wave = __builtin_amdgcn_readfirstlane(tid >> 6);
  const int b = blockIdx.x;
  if (b < B_MOD || (b >= B_PO && b < B_STAT)) {
    const bool isq = b < B_MOD;
    const int t = isq ? (b >> 3) * 8 + wave : (b - B_PO) * 8 + wave;
    if (!isq && t >= 162) return;
    const int kap = t % 18, grp = t / 18, hg = b & 7;
    char* img = sm + wave * (32 * 160);
    const int ch = lane & 7;
    const u16* src;
    if (isq) {
      int c8 = grp * 8 + ch; if (c8 > 26) c8 = 26;
      src = wqkv + (size_t)(kap * 32 + (lane >> 3)) * 1728 + (c8 / 9) * 576 + hg * 72 + (c8 % 9) * 8;
    } else {
      src = wo + (size_t)(kap * 32 + (lane >> 3)) * D_ + grp * 64 + ch * 8;
    }
    const int pitch8 = isq ? 8 * 1728 : 8 * D_;
    u32x4 v[4];
#pragma unroll
    for (int i = 0; i < 4; ++i) v[i] = *(const u32x4*)(src + i * pitch8);
#pragma unroll
    for (int i = 0; i < 4; ++i) *(u32x4*)(img + (i * 8 + (lane >> 3)) * 160 + ch * 16) = v[i];
    asm volatile("s_waitcnt lgkmcnt(0)" ::: "memory");
    const unsigned a0 = (unsigned)(uintptr_t)(LAS char*)img + (8 * (lane >> 4) + ((lane & 15) >> 2)) * 160 + 8 * (lane & 3);
    char* dst = isq ? ws + WS_WQF + (size_t)(hg * 14 * 18) * 1024 : ws + WS_WOF;
#pragma unroll
    for (int f = 0; f < 4; ++f) {
      const bf16x8 fr = tr_frag(a0 + f * 32, a0 + f * 32 + 4 * 160);
      const int nf = grp * 4 + f;
      if (!isq || nf < 14) *(bf16x8*)(dst + (size_t)(nf * 18 + kap) * 1024 + lane * 16) = fr;
    }
    return;
  }
  if (b < B_PO) {
    const int j = b - B_MOD, cg = ((j >> 4) * 8 + (j & 7)) * 2 + ((j >> 3) & 1);
    if (cg >= 108) return;
    const int rg = tid >> 2, cl = tid & 3;
    const rsrc_t wr = mk_rsrc(wmod, 576 * 3456 * 2), cr = mk_rsrc(cvec, 576 * 2);
    u32x4 w[5]; unsigned cv[5];
#pragma unroll
    for (int i = 0; i < 5; ++i) {
      w[i] = ld16(wr, (unsigned)(rg * 3456 + cl * 8) * 2u, (unsigned)(i * 128 * 3456 + cg * 32) * 2u);
      cv[i] = __builtin_amdgcn_raw_buffer_load_b16(cr, rg * 2, i * 256, 0);
    }
    float acc[8] = {};
#pragma unroll
    for (int i = 0; i < 5; ++i) {
      const float s = rbf(silu_f(bflo(cv[i])));
#pragma unroll
      for (int q = 0; q < 4; ++q) { acc[2 * q] += s * bflo(w[i][q]); acc[2 * q + 1] += s * bfhi(w[i][q]); }
    }
#pragma unroll
    for (int q = 0; q < 8; ++q) { acc[q] += __shfl_xor(acc[q], 4); acc[q] += __shfl_xor(acc[q], 8); acc[q] += __shfl_xor(acc[q], 16); acc[q] += __shfl_xor(acc[q], 32); }
    float* red = (float*)sm;
    if (lane < 4) {
#pragma unroll
      for (int q = 0; q < 8; ++q) red[wave * 32 + lane * 8 + q] = acc[q];
    }
    __syncthreads();
    if (tid < 32) {
      float v = 0.f;
#pragma unroll
      for (int q = 0; q < 8; ++q) v += red[q * 32 + tid];
      v = rbf(v);
      const int vec = cg / 18;
      if (vec == 1 || vec == 4) v = rbf(1.f + v);
      ((float*)(ws + WS_MODF))[cg * 32 + tid] = v;
      if (vec < 2) ((u16*)(ws + WS_MODB))[((cg - vec * 18) * 32 + tid) * 2 + vec] = (u16)(__float_as_uint(v) >> 16);
    }
    return;
  }
  if (b < B_CODE) {
    const int row = (b & 7) * 256 + (((b - B_STAT) >> 3) * 8 + wave) * 8 + (lane >> 3), cl = lane & 7;
    u32x4 v[9];
#pragma unroll
    for (int i = 0; i < 9; ++i) v[i] = *(const u32x4*)(x + (size_t)row * D_ + (cl + 8 * i) * 8);
    float s = 0.f;
#pragma unroll
    for (int i = 0; i < 9; ++i)
#pragma unroll
      for (int j = 0; j < 4; ++j) s += bflo(v[i][j]) + bfhi(v[i][j]);
    s += __shfl_xor(s, 1); s += __shfl_xor(s, 2); s += __shfl_xor(s, 4);
    const float mean = s * (1.f / D_);
    float q = 0.f;
#pragma unroll
    for (int i = 0; i < 9; ++i)
#pragma unroll
      for (int j = 0; j < 4; ++j) { const float a = bflo(v[i][j]) - mean, c = bfhi(v[i][j]) - mean; q += a * a + c * c; }
    q += __shfl_xor(q, 1); q += __shfl_xor(q, 2); q += __shfl_xor(q, 4);
    const float rstd = __builtin_amdgcn_rsqf(q * (1.f / D_) + 1e-5f);
    const float nm = -mean * rstd;
#pragma unroll
    for (int i = 0; i < 9; ++i) {
      u32x4 o;
#pragma unroll
      for (int j = 0; j < 4; ++j) o[j] = pack_bf16(bflo(v[i][j]) * rstd + nm, bfhi(v[i][j]) * rstd + nm);
      *(u32x4*)(ws + WS_N1 + ((size_t)row * D_ + (cl + 8 * i) * 8) * 2) = o;
    }
    return;
  }
  if (b < B_FLAG) {
    unsigned clo, chi, elo, ehi;
    asm volatile("s_getpc_b64 s[20:21]\n\ts_add_u32 %0, s20, k_main@rel32@lo+4\n\ts_addc_u32 %1, s21, k_main@rel32@hi+12"
                 : "=s"(clo), "=s"(chi)::"s20", "s21", "scc");
    asm volatile("s_getpc_b64 s[20:21]\n\ts_add_u32 %0, s20, k_tail@rel32@lo+4\n\ts_addc_u32 %1, s21, k_tail@rel32@hi+12"
                 : "=s"(elo), "=s"(ehi)::"s20", "s21", "scc");
    const GAS char* code = (const GAS char*)(((u64)chi << 32) | clo);
    const long long len = (long long)(((u64)ehi << 32) | elo) - (long long)(((u64)chi << 32) | clo);
    unsigned acc = 0;
#pragma unroll
    for (int i = 0; i < 4; ++i) {
      const int o = (i * 512 + tid) * 64;
      if (o + 4 <= len) acc += *(const GAS unsigned*)(code + o);
    }
    asm volatile("" ::"v"(acc));
    return;
  }
  u32x4* fl = (u32x4*)(ws + WS_FLAGS);
  for (int i = (b - B_FLAG) * 512 + tid; i < FLAG_WORDS / 4; i += 8 * 512) fl[i] = (u32x4){0u, 0u, 0u, 0u};
}

__global__ __launch_bounds__(512) void k_pre(const u16* __restrict__ x, const u16* __restrict__ wqkv, const u16* __restrict__ wo,
                                             const u16* __restrict__ cvec, const u16* __restrict__ wmod, char* __restrict__ ws) {
  k_pre_body(x, wqkv, wo, cvec, wmod, ws);
}

namespace mlp {
constexpr int NBUF = 8, SLOT = 18432, PF = 3, NPRE = 5;
constexpr int RING_BYTES = NBUF * SLOT;
constexpr int OWN = 1 * SLOT;
constexpr int NU1 = 21, NU2 = 9, NU = NU1 + NU2;
constexpr int NS1 = 324, NS2 = 162, NS = NS1 + NS2;
enum { OP_NONE = 0, OP_W, OP_B, OP_POLL, OP_PF };
#define TA_COST 28
constexpr int CHK_DELAY = 16;
constexpr int slot_of(int U) { int u = U % NBUF; return u < 3 ? 5 + u : u < 5 ? u : u - 5; }
constexpr int unit_chunk(int U) { return U < 9 ? 0 : U < 15 ? 1 : U < 21 ? 2 : 3; }
constexpr int unit_np(int U) { return unit_chunk(U) == 2 ? 12 : 18; }
constexpr int unit_nk(int U) { int c = unit_chunk(U); return (c == 0 || c == 3) ? 2 : 3; }
constexpr int unit_k0(int U) { return U < 9 ? 2 * U : U < 15 ? 3 * (U - 9) : U < 21 ? 3 * (U - 15) : 2 * (U - 21); }
constexpr int chunk_nt(int c) { return c == 0 ? 4 : c == 1 ? 3 : 2; }
constexpr int chunk_t0(int c) { return c == 0 ? 0 : c == 1 ? 4 : 7; }
constexpr int tile_set(int i) { return i < 4 ? i : i < 7 ? i - 4 : i == 7 ? 3 : 0; }
constexpr int run_a0(int l) { return l == 0 ? 0 : l == 1 ? 3 : l == 2 ? 2 : 5; }
constexpr int run_c1(int l) { return l == 0 ? 4 : l == 1 ? 0 : l == 2 ? 6 : 0; }
constexpr int run_e0(int l) { return l == 0 ? 7 : l == 1 ? 7 : l == 2 ? 0 : 3; }
constexpr int pos_row(int pos) {
  if (pos < 16) return (pos >> 2) * 144 + 16 * (run_a0(pos >> 2) + (pos & 3));
  if (pos < 28) return ((pos - 16) / 3) * 144 + 16 * (run_c1((pos - 16) / 3) + (pos - 16) % 3);
  return ((pos - 28) >> 1) * 144 + 16 * (run_e0((pos - 28) >> 1) + ((pos - 28) & 1));
}
struct Sched {
  short unit[NS], ub[NS], kl[NS], bi[NS], sub[NS], tile[NS], first[NS];
  short blk_u0[NS], blk_nu[NS], bar_wait[NS];
  short op_t[NS], op_a[NS], op_b[NS];
  short pro_n, pro_a[16], pro_b[16];
  short bwait[NS];
  short eA[NS], eB[NS], eC[NS];
  short pub[NS], pub_wait[NS], chk[NS], chk_wait[NS];
  short pst[NS];
  bool ok;
  short min_lead_b, min_lead_w;
};
constexpr Sched make_sched() {
  Sched s{};
  s.ok = true; s.min_lead_b = 1000; s.min_lead_w = 1000;
  int G = 0;
  int blocks[26][2] = {{0,1},{1,1},{2,1},{3,1},{4,1},{5,1},{6,1},{7,2},{9,2},{11,1},{12,1},{13,2},{15,2},{17,1},{18,1},{19,2},
                       {21,1},{22,1},{23,1},{24,1},{25,1},{26,1},{27,3},{-1,0}};
  int blk_of_unit[NU] = {};
  for (int i = 0; i < NS; ++i) { s.blk_u0[i] = -1; s.bar_wait[i] = -1; s.bwait[i] = -1; s.eA[i] = -1; s.eB[i] = -1; s.eC[i] = -1; s.pst[i] = -1; }
  for (int B = 0; blocks[B][0] >= 0; ++B) {
    int u0 = blocks[B][0], nu = blocks[B][1], c = unit_chunk(u0);
    s.blk_u0[G] = (short)u0; s.blk_nu[G] = (short)nu;
    for (int k = 0; k < nu; ++k) blk_of_unit[u0 + k] = B;
    if (c < 3) {
      for (int t = 0; t < chunk_nt(c); ++t)
        for (int k = 0; k < nu; ++k)
          for (int kl = 0; kl < unit_nk(u0); ++kl)
            for (int m = 0; m < 2; ++m) {
              s.unit[G] = (short)(u0 + k); s.ub[G] = (short)k; s.kl[G] = (short)kl; s.bi[G] = (short)(unit_k0(u0 + k) + kl);
              s.sub[G] = (short)m; s.tile[G] = (short)(chunk_t0(c) + t); s.first[G] = (short)(s.bi[G] == 0 && m == 0); ++G;
            }
    } else if (nu == 1) {
      for (int kl = 0; kl < 2; ++kl)
        for (int nf = 0; nf < 9; ++nf) {
          s.unit[G] = (short)u0; s.ub[G] = 0; s.kl[G] = (short)kl; s.bi[G] = (short)(unit_k0(u0) + kl);
          s.tile[G] = (short)nf; s.first[G] = (short)(s.bi[G] == 0); ++G;
        }
    } else {
      for (int nf = 0; nf < 9; ++nf)
        for (int k = 0; k < nu; ++k)
          for (int kl = 0; kl < 2; ++kl) {
            s.unit[G] = (short)(u0 + k); s.ub[G] = (short)k; s.kl[G] = (short)kl; s.bi[G] = (short)(unit_k0(u0 + k) + kl);
            s.tile[G] = (short)nf; ++G;
            if (nf > 0 && k == 0 && kl == 1) s.pst[G - 1] = (short)(nf - 1);
          }
    }
  }
  int store_at[NS] = {}, last_store[3] = {};
  int prev = 0;
  for (int i = 0; i < 9; ++i) {
    int R = 0, D = 100000;
    for (int g = 0; g < NS1; ++g) if (s.tile[g] == i) R = g + 1;
    for (int g = R; g < NS1; ++g) if (tile_set(s.tile[g]) == tile_set(i)) { D = g; break; }
    int st = R > prev ? R : prev;
    int sp = (st + 14 < D) ? 2 : 1;
    if (st + sp * 7 >= D) s.ok = false;
    for (int j = 0; j < 8; ++j) {
      int g = st + sp * j;
      s.eA[g] = (short)(i * 8 + j); s.eB[g + 1] = (short)(i * 8 + j); s.eC[g + 2] = (short)(i * 8 + j);
      if (j == 7) { store_at[g + 2] += 1; last_store[i < 4 ? 0 : i < 7 ? 1 : 2] = g + 2; }
    }
    prev = st + sp * 8;
  }
  const int poll_at[3] = {236, 326, 388};
  for (int c = 0; c < 3; ++c) s.pub[last_store[c] + (c == 2 ? 10 : 14)] = (short)(c + 1);
  int xlast[18] = {};
  for (int g = 0; g < NS1; ++g) xlast[s.bi[g]] = g;
  int ba[96] = {}, bb[96] = {}, be[96] = {}, nb = 0;
  for (int k = 0; k < 18; ++k) for (int m = 0; m < 2; ++m) { ba[nb] = k; bb[nb] = m; be[nb] = 0; ++nb; }
  for (int c = 0; c < 3; ++c) {
    int k0 = c == 0 ? 0 : c == 1 ? 8 : 14, k1 = c == 0 ? 8 : c == 1 ? 14 : 18;
    for (int k = k0; k < k1; ++k) for (int m = 0; m < 2; ++m) {
      int e = poll_at[c] + CHK_DELAY + 1; if (xlast[k] + 1 > e) e = xlast[k] + 1;
      ba[nb] = 18 + k; bb[nb] = m; be[nb] = e; ++nb;
    }
  }
  int wa[96] = {}, wb[96] = {}, nw = 0;
  for (int U = NPRE; U < NU; ++U) for (int j = 0; j < (unit_np(U) + 7) / 8; ++j) { wa[nw] = U; wb[nw] = j; ++nw; }
  int bi = 0, wi = 0;
  while (bi < 8) { s.pro_a[s.pro_n] = (short)ba[bi]; s.pro_b[s.pro_n] = (short)bb[bi]; ++s.pro_n; ++bi; }
  int wneed[NU] = {}, bneed[36] = {};
  for (int g = NS - 1; g >= 0; --g) {
    bneed[(g >= NS1 ? 18 : 0) + s.bi[g]] = g;
    if (s.blk_u0[g] >= 0) for (int k = 0; k < s.blk_nu[g]; ++k) wneed[s.blk_u0[g] + k] = g - PF;
  }
  {
    int bars_done = 0, ta_free = 0;
    for (int g = 0; g < NS; ++g) {
      int gn = g + PF;
      if (gn < NS && s.blk_u0[gn] >= 0) bars_done = blk_of_unit[s.blk_u0[gn]];
      bool polled = false;
      for (int c = 0; c < 3; ++c) if (poll_at[c] == g) { s.op_t[g] = OP_POLL; s.op_a[g] = (short)c; s.chk[g + CHK_DELAY] = (short)(c + 1); polled = true; }
      for (int c = 0; c < 3; ++c) if (g == 151 + 3 * c) { s.op_t[g] = OP_PF; s.op_a[g] = (short)c; polled = true; }
      if (polled || ta_free > g * 10 + 5) continue;
      const int base = ta_free > g * 10 - 5 ? ta_free : g * 10 - 5;
      bool wready = wi < nw && !(wa[wi] - NBUF >= 0 && blk_of_unit[wa[wi] - NBUF] > bars_done - 2);
      bool bready = bi < nb && g >= be[bi];
      int bdl = bready ? bneed[ba[bi]] : 1000000;
      int wdl = wready ? wneed[wa[wi]] - 12 : 1000000;
      if (bready && bdl <= wdl) {
        s.op_t[g] = OP_B; s.op_a[g] = (short)ba[bi]; s.op_b[g] = (short)bb[bi];
        ta_free = base + 8 * TA_COST / 10;
        if (bneed[ba[bi]] - g < s.min_lead_b) s.min_lead_b = (short)(bneed[ba[bi]] - g);
        ++bi;
      } else if (wready) {
        s.op_t[g] = OP_W; s.op_a[g] = (short)wa[wi]; s.op_b[g] = (short)wb[wi];
        const int c = unit_chunk(wa[wi]);
        int pieces = unit_np(wa[wi]) - 8 * wb[wi]; if (pieces > 8) pieces = 8;
        ta_free = base + (c == 0 ? 8 : c == 3 ? 11 : c == 1 ? 14 : 20) * TA_COST * pieces / 80;
        if (wneed[wa[wi]] - g < s.min_lead_w) s.min_lead_w = (short)(wneed[wa[wi]] - g);
        ++wi;
      }
    }
    if (bi < nb || wi < nw || s.min_lead_b <= 2 || s.min_lead_w <= 2) s.ok = false;
  }
  {
    int n = 0, wpos[NU] = {}, bpos[36] = {}, pollpos = 0, storepos = 0;
    bool bseen[36] = {};
    for (int i = 0; i < s.pro_n; ++i) { ++n; bpos[s.pro_a[i]] = n; }
    s.bar_wait[0] = (short)n;
    for (int g = 0; g < NS; ++g) {
      int gn = g + PF;
      if (gn < NS && s.blk_u0[gn] >= 0) {
        int need = 0;
        for (int k = 0; k < s.blk_nu[gn]; ++k) if (wpos[s.blk_u0[gn] + k] > need) need = wpos[s.blk_u0[gn] + k];
        s.bar_wait[gn] = (short)(n - need);
      }
      if (s.op_t[g] == OP_POLL) { ++n; pollpos = n; }
      if (s.op_t[g] == OP_PF) ++n;
      if (s.op_t[g] == OP_B) { ++n; bpos[s.op_a[g]] = n; }
      if (s.op_t[g] == OP_W) { if ((s.op_b[g] + 1) * 8 <= unit_np(s.op_a[g])) ++n; wpos[s.op_a[g]] = n; }
      if (s.chk[g]) s.chk_wait[g] = (short)(n - pollpos);
      int bk = (g >= NS1 ? 18 : 0) + s.bi[g];
      if (!bseen[bk]) { bseen[bk] = true; s.bwait[g] = (short)(n - bpos[bk]); }
      if (store_at[g]) { n += store_at[g]; storepos = n; }
      if (s.pub[g]) { s.pub_wait[g] = (short)(n - storepos); ++n; }
    }
  }
  for (int g = 0; g < NS; ++g) if (s.bar_wait[g] > 63 || s.bwait[g] > 63 || s.pub_wait[g] > 63 || s.chk_wait[g] > 63) s.ok = false;
  return s;
}
constexpr Sched SC = make_sched();
static_assert(SC.ok, "schedule");
}

namespace p1 {
constexpr int XS = 0;
constexpr int MODS = 92160;
constexpr int QP = 456;
constexpr int OS = 40960;
constexpr int OP = 160;
}
namespace p2 {
constexpr int OSM = 0;
constexpr int RED = 36864;
constexpr int RST = RED + 32 * 37 * 8;
constexpr int X1S = mlp::RING_BYTES;
constexpr int MV = X1S + 9216;
}
constexpr int SCRATCH = p2::MV + 4 * 144 * 4;
constexpr int LDS_BYTES = SCRATCH + 1024;

DI void mem_fence() { asm volatile("" ::: "memory"); }

template <int NN, int M0>
DI void p1_gemm(char* smem, bf16x8 (&wf)[2][18], rsrc_t wr, unsigned wso, rsrc_t wor, unsigned wo0, f32x4 (&acc)[2][5], int lane) {
  const int g = lane >> 4, m16 = lane & 15;
  const char* xs0 = smem + p1::XS + (g * 16 + (m16 ^ (2 * g))) * 16;
  const char* xs1 = smem + p1::XS + (g * 16 + (m16 ^ (2 * g + 1))) * 16;
  static_for<18>([&](auto kc) LAMBDA_INLINE {
    constexpr int k = decltype(kc)::value;
    if constexpr (k + 6 < 18) {
#pragma unroll
      for (int n = 0; n < NN; ++n) wf[n][k + 6] = ldfrag(wr, lane * 16, wso + (n * 18 + k + 6) * 1024);
    }
    if constexpr (k >= 12) {
      wf[0][2 * (k - 12)] = ldfrag(wor, lane * 16, wo0 + 2 * (k - 12) * 1024);
      wf[0][2 * (k - 12) + 1] = ldfrag(wor, lane * 16, wo0 + (2 * (k - 12) + 1) * 1024);
    }
    bf16x8 bf[5];
#pragma unroll
    for (int m = M0; m < 5; ++m) bf[m] = *(const bf16x8*)(((k & 1) ? xs1 : xs0) + (k * 5 + m) * 1024);
#pragma unroll
    for (int n = 0; n < NN; ++n)
#pragma unroll
      for (int m = M0; m < 5; ++m) acc[n][m] = __builtin_amdgcn_mfma_f32_16x16x32_bf16(wf[n][k], bf[m], acc[n][m], 0, 0, 0);
    mem_fence();
  });
}

extern "C" __global__ __launch_bounds__(512, 2) void k_main(const u16* __restrict__ x, const u16* __restrict__ w1, const u16* __restrict__ w3,
                                                 const u16* __restrict__ w2, char* __restrict__ ws, u16* __restrict__ out) {
  using namespace mlp;
  extern __shared__ __attribute__((aligned(16))) char smem[];
  const int tid = threadIdx.x, lane = tid & 63;
  const int wave = __builtin_amdgcn_readfirstlane(tid >> 6);
  const int b = blockIdx.x, xcd = b & 7, idx = b >> 3;
  const int mt = idx >> 2, ftl = idx & 3;
  const unsigned lds0 = (unsigned)(uintptr_t)(LAS char*)smem;
  unsigned* flags = (unsigned*)(ws + WS_FLAGS);
  STAMP_AT(0);
  const int a0 = ftl == 0 ? 0 : ftl == 1 ? 3 : ftl == 2 ? 2 : 5;
  const int c1 = ftl == 0 ? 4 : ftl == 1 ? 0 : ftl == 2 ? 6 : 0;
  const int e0 = ftl == 0 ? 7 : ftl == 1 ? 7 : ftl == 2 ? 0 : 3;
  const int fbase = (xcd * 4 + ftl) * 144;

  const char* sp[3];
  unsigned so[3];
  auto stage_setup = [&](auto cc_) LAMBDA_INLINE {
    constexpr int c = decltype(cc_)::value;
    const int lane = lane_now();
#pragma unroll
    for (int j = 0; j < (c == 2 ? 2 : 3); ++j) {
      int i = wave + 8 * j;
      if (c == 2) { if (i > 11) i = 11; } else { if (i > 17) i = 17; }
      const int ci = i * 64 + lane;
      if constexpr (c == 0) {
        int row = ci / 18, cc = ci % 18; if (cc > 15) cc = 15;
        sp[j] = (const char*)((cc >= 8 ? w3 : w1) + (size_t)row * F_ + fbase + a0 * 16 + (cc & 7) * 8);
      } else if constexpr (c == 1) {
        int row = ci / 12, cc = ci % 12;
        sp[j] = (const char*)((cc >= 6 ? w3 : w1) + (size_t)row * F_ + fbase + c1 * 16 + (cc >= 6 ? cc - 6 : cc) * 8);
      } else if constexpr (c == 2) {
        int row = ci >> 3, cc = ci & 7, pos = (cc >> 1) ^ ((row >> 1) & 1);
        sp[j] = (const char*)((pos >= 2 ? w3 : w1) + (size_t)row * F_ + fbase + e0 * 16 + (pos & 1) * 16 + (cc & 1) * 8);
      } else {
        int ip = i >= 9 ? i - 9 : i, c2 = ip * 64 + lane, row = c2 / 18, cc = c2 % 18;
        so[j] = (unsigned)(row * D_ + cc * 8) * 2u;
      }
    }
  };
  const u16* w2b = w2 + (size_t)xcd * 576 * D_ + ftl * 144;
  auto stage_round = [&](auto Uc, auto jc) LAMBDA_INLINE {
    constexpr int U = decltype(Uc)::value, j = decltype(jc)::value, c = unit_chunk(U);
    if constexpr (j == 0 && unit_k0(U) == 0) stage_setup(IC(c));
    const unsigned dst = lds0 + slot_of(U) * SLOT + (wave + 8 * j) * 1024;
    constexpr bool all = (j + 1) * 8 <= unit_np(U);
    if (all || wave + 8 * j < unit_np(U)) {
      if constexpr (c < 3) {
        glds16v(sp[j], dst);
      } else {
        constexpr int pA = 2 * unit_k0(U), pB = pA + 2;
        const bool h1 = wave + 8 * j >= 9;
        const int r0 = h1 ? pos_row(pB) : pos_row(pA);
        constexpr int dA = pos_row(pA + 1) - pos_row(pA) - 16, dB = pos_row(pB + 1) - pos_row(pB) - 16;
        unsigned vo = so[j];
        if constexpr (dA != 0 || dB != 0) vo += (so[j] >= 16u * D_ * 2u) ? (unsigned)((h1 ? dB : dA) * D_ * 2) : 0u;
        glds16(vo, w2b + (size_t)r0 * D_, dst);
      }
    }
    if constexpr (c < 3) sp[j] += (size_t)unit_nk(U) * 32 * F_ * 2;
  };

  const int g = lane >> 4, m16 = lane & 15;
  bf16x8 wf[2][18];
  {
    using namespace p1;
    const int rt = xcd * 4 + (idx >> 3), hg = idx & 7;
    const int r0 = rt * 64 - 16;
    const rsrc_t xr = mk_rsrc(ws + WS_N1, S_ * D_ * 2), mr = mk_rsrc(ws + WS_MODF, 65536 + 576 * 4);
    const rsrc_t wr = mk_rsrc(ws + WS_WQF, 8 * 14 * 18 * 1024);
    const int r8 = lane >> 3, ch = lane & 7;
    u32x4 sa[2][2];
#pragma unroll
    for (int part = 0; part < 2; ++part) {
      const unsigned so_ = 65536 + (part ? 8 : wave) * 256;
      sa[part][0] = ld16(mr, ch * 32, so_); sa[part][1] = ld16(mr, ch * 32 + 16, so_);
    }
    u32x4 xv[12];
#pragma unroll
    for (int i = 0; i < 12; ++i) {
      const int rg = i < 10 ? i : i == 10 ? wave : 8 + (wave & 1), ln = i < 10 ? wave : 8;
      if (i < 11 || wave < 2) xv[i] = ld16(xr, r8 * 1152 + ch * 16, (unsigned)((r0 + rg * 8) * 1152 + ln * 128));
    }
    f32x4 mvv;
    if (wave < 3) mvv = ldf4(mr, tid < 144 ? ((2 + tid / 36) * D_ + (tid % 36) * 4) * 4 : 0x40000000, ftl * 576);
    barrier_raw();
    STAMP_AT(17);
    const int nf0 = wave < 4 ? 4 + 2 * wave : wave < 6 ? 2 * (wave - 4) : 6 + wave;
    const unsigned wso = (unsigned)((hg * 14 + nf0) * 18 * 1024);
    auto w_load = [&](int k) LAMBDA_INLINE {
      wf[0][k] = ldfrag(wr, lane * 16, wso + k * 1024);
      if (wave < 6) wf[1][k] = ldfrag(wr, lane * 16, wso + (18 + k) * 1024);
      mem_fence();
    };
    w_load(0); w_load(1);
    STAMP_AT(18);
    {
      const int gq = ch & 3, kb = ch >> 2, swz = 2 * gq + kb;
#pragma unroll
      for (int i = 0; i < 12; ++i) {
        const int part = i >= 10, ln = part ? 8 : wave;
        const int rg = i < 10 ? i : i == 10 ? wave : 8 + (wave & 1);
        if (i < 11 || wave < 2) {
          u32x4 o;
#pragma unroll
          for (int j = 0; j < 4; ++j) {
            const unsigned m0 = sa[part][j >> 1][(2 * j) & 3], m1 = sa[part][j >> 1][(2 * j + 1) & 3];
            o[j] = pack_bf16(bflo(xv[i][j]) * bfhi(m0) + bflo(m0), bfhi(xv[i][j]) * bfhi(m1) + bflo(m1));
          }
          *(u32x4*)(smem + XS + ((2 * ln + kb) * 5 + (rg >> 1)) * 1024 + gq * 256 + (((rg & 1) * 8 + r8) ^ swz) * 16) = o;
        }
        if (i < 8 && (i & 1)) w_load(2 + (i >> 1));
      }
    }
    if (tid < 144) *(f32x4*)(smem + p2::MV + tid * 16) = mvv;
    STAMP_AT(19);
    lds_barrier();
    STAMP_AT(1);
    const rsrc_t wor = mk_rsrc(ws + WS_WOF, 36 * 18 * 1024);
    const unsigned wo0 = (unsigned)((ftl * 9 + wave) * 18 * 1024), wo8 = (unsigned)((ftl * 9 + 8) * 18 * 1024);
    f32x4 acc[2][5] = {};
    if (wave < 4) p1_gemm<2, 0>(smem, wf, wr, wso, wor, wo0, acc, lane);
    else if (wave < 6) p1_gemm<2, 1>(smem, wf, wr, wso, wor, wo0, acc, lane);
    else {
      p1_gemm<1, 0>(smem, wf, wr, wso, wor, wo0, acc, lane);
    }
    STAMP_AT(20);
    lds_barrier();
    STAMP_AT(2);
#pragma unroll
    for (int n = 0; n < 2; ++n)
#pragma unroll
      for (int m = 0; m < 5; ++m) {
        if (n == 1 && wave >= 6) continue;
        u32x2 v = {pack_bf16(acc[n][m][0], acc[n][m][1]), pack_bf16(acc[n][m][2], acc[n][m][3])};
        *(u32x2*)(smem + (m * 16 + m16) * QP + ((nf0 + n) * 16 + 4 * g) * 2) = v;
      }
    lds_barrier();
    STAMP_AT(21);
    auto bulk = [&](auto ic) LAMBDA_INLINE {
      constexpr int i = decltype(ic)::value;
      if constexpr (i < 2) {
#pragma unroll
        for (int k = 12 + 3 * i; k < 15 + 3 * i; ++k) wf[0][k] = ldfrag(wor, lane * 16, wo0 + k * 1024);
      } else if constexpr (i < 8) {
        if (wave < 2) {
#pragma unroll
          for (int k = 3 * (i - 2); k < 3 * (i - 1); ++k) wf[1][k] = ldfrag(wor, lane * 16, wo8 + k * 1024);
        }
      }
      mem_fence();
    };
    auto bulk2 = [&](int w) LAMBDA_INLINE {
      static_for<12>([&](auto wc) LAMBDA_INLINE {
        constexpr int ww = decltype(wc)::value;
        if (w == ww) { if constexpr (ww < 3) { bulk(IC(2 + 2 * ww)); bulk(IC(3 + 2 * ww)); } else bulk(IC(5 + ww)); }
      });
    };
    {
      typedef __bf16 bf2 __attribute__((ext_vector_type(2)));
      const int rl = (wave >> 1) * 16 + m16, hd = (wave & 1) * 2 + (g & 1), hw = lane >> 5;
      const int grow = rt * 64 + rl;
      const unsigned* qrow = (const unsigned*)(smem + (rl + 16) * QP) + hd * 9;
      unsigned qv[9];
#pragma unroll
      for (int d = 0; d < 9; ++d) qv[d] = qrow[d];
      bulk(IC(0)); bulk(IC(1));
      float lg[4];
#pragma unroll
      for (int w = 0; w < 4; ++w) {
        const int ww = hw * 4 + w;
        const unsigned* kr = (const unsigned*)(smem + (rl + 16 - 2 * ww) * QP) + 36 + hd * 9;
        float a = 0.f;
#pragma unroll
        for (int d = 0; d < 9; ++d) a = __builtin_amdgcn_fdot2_f32_bf16(__builtin_bit_cast(bf2, qv[d]), __builtin_bit_cast(bf2, kr[d]), a, false);
        a *= 0.23570226039551584f;
        lg[w] = (grow - 2 * ww >= 0) ? a : -1e9f;
        bulk2(w);
      }
      STAMP_AT(22);
      float mxv = fmaxf(fmaxf(lg[0], lg[1]), fmaxf(lg[2], lg[3]));
      mxv = fmaxf(mxv, __shfl_xor(mxv, 32));
      float pw[8], sum = 0.f;
#pragma unroll
      for (int w = 0; w < 4; ++w) { lg[w] = __builtin_amdgcn_exp2f((lg[w] - mxv) * 1.44269504089f); sum += lg[w]; }
      sum += __shfl_xor(sum, 32);
      const float inv = __builtin_amdgcn_rcpf(sum);
#pragma unroll
      for (int w = 0; w < 4; ++w) {
        const float mine = lg[w] * inv, other = __shfl_xor(mine, 32);
        pw[w] = hw ? other : mine; pw[4 + w] = hw ? mine : other;
      }
      float o[10] = {};
#pragma unroll
      for (int w = 0; w < 8; ++w) {
        const unsigned* vr = (const unsigned*)(smem + (rl + 16 - 2 * w) * QP) + 72 + hd * 9 + hw * 5;
#pragma unroll
        for (int d = 0; d < 5; ++d) { const unsigned v = vr[d]; o[2 * d] += pw[w] * bflo(v); o[2 * d + 1] += pw[w] * bfhi(v); }
        bulk2(4 + w);
      }
      STAMP_AT(23);
      unsigned* orow = (unsigned*)(smem + OS + rl * OP) + hd * 9 + hw * 5;
#pragma unroll
      for (int d = 0; d < 5; ++d) if (d < 4 || hw == 0) orow[d] = pack_bf16(o[2 * d], o[2 * d + 1]);
    }
    lds_barrier();
    STAMP_AT(3);
    const rsrc_t ors = mk_rsrc(ws + WS_OF, S_ * D_ * 2);
#pragma unroll
    for (int it = 0; it < 2; ++it) {
      const int pc = tid + it * 512;
      if (pc < 576) {
        const int row = pc & 63, c = pc >> 6;
        const u32x4 v = *(const u32x4*)(smem + OS + row * OP + c * 16);
        const int c8 = hg * 9 + c, mf = rt * 4 + (row >> 4);
        st16_sc1(ors, (unsigned)(((mf * 18 + (c8 >> 2)) * 64 + (c8 & 3) * 16 + (row & 15)) * 16), 0, v);
      }
    }
    wait_vm<0>();
    barrier_raw();
    if (tid == 0) set_flag(flags + FL_O + rt * 32 + hg, 1u);
    STAMP_AT(4);
  }

  {
    using namespace p2;
    const int rt = mt * 8 + xcd, nt = ftl;
    u32x2 xr[3];
#pragma unroll
    for (int t = 0; t < 3; ++t) {
      const int nf = t < 2 ? wave : 8, mi = t < 2 ? t : (wave & 1);
      xr[t] = *(const u32x2*)(x + (size_t)(rt * 32 + mi * 16 + m16) * D_ + nt * 144 + nf * 16 + 4 * g);
    }
    wait_flags(flags + FL_O + (rt >> 1) * 32, 8, 1u, lane);
    STAMP_AT(5);
    const rsrc_t ors = mk_rsrc(ws + WS_OF, S_ * D_ * 2);
    u32x4 ov[5];
#pragma unroll
    for (int i = 0; i < 5; ++i) {
      const int f = wave + 8 * i;
      if (f < 36) ov[i] = ld16_sc1(ors, lane * 16, (unsigned)((rt * 36 + f) * 1024));
    }
#pragma unroll
    for (int i = 0; i < 5; ++i) {
      const int f = wave + 8 * i;
      if (f < 36) *(u32x4*)(smem + OSM + f * 1024 + lane * 16) = ov[i];
    }
    lds_barrier();
    STAMP_AT(6);
    f32x4 acc[3] = {};
    bf16x8 ob[2][18];
#pragma unroll
    for (int k = 0; k < 3; ++k) { ob[0][k] = *(const bf16x8*)(smem + OSM + k * 1024 + lane * 16); ob[1][k] = *(const bf16x8*)(smem + OSM + (18 + k) * 1024 + lane * 16); }
    static_for<18>([&](auto kc) LAMBDA_INLINE {
      constexpr int k = decltype(kc)::value;
      if constexpr (k + 3 < 18) {
        ob[0][k + 3] = *(const bf16x8*)(smem + OSM + (k + 3) * 1024 + lane * 16);
        ob[1][k + 3] = *(const bf16x8*)(smem + OSM + (21 + k) * 1024 + lane * 16);
      }
      acc[0] = __builtin_amdgcn_mfma_f32_16x16x32_bf16(wf[0][k], ob[0][k], acc[0], 0, 0, 0);
      acc[1] = __builtin_amdgcn_mfma_f32_16x16x32_bf16(wf[0][k], ob[1][k], acc[1], 0, 0, 0);
      if (wave < 2) acc[2] = __builtin_amdgcn_mfma_f32_16x16x32_bf16(wf[1][k], wave ? ob[1][k] : ob[0][k], acc[2], 0, 0, 0);
      if constexpr (k < 15) stage_round(IC(k / 3), IC(k % 3));
    });
    float2* red = (float2*)(smem + RED);
    const float* mv = (const float*)(smem + MV);
    f32x4 xv[3];
#pragma unroll
    for (int t = 0; t < 3; ++t) {
      if (t == 2 && wave >= 2) continue;
      const int nf = t < 2 ? wave : 8, mi = t < 2 ? t : (wave & 1);
      const f32x4 gv = *(const f32x4*)(mv + nf * 16 + 4 * g);
      u32x2 xb = {pack_bf16(bflo(xr[t][0]) + gv[0] * acc[t][0], bfhi(xr[t][0]) + gv[1] * acc[t][1]),
                  pack_bf16(bflo(xr[t][1]) + gv[2] * acc[t][2], bfhi(xr[t][1]) + gv[3] * acc[t][3])};
      *(u32x2*)(smem + X1S + ((nf * 2 + mi) * 64 + lane) * 8) = xb;
      f32x4 v = {bflo(xb[0]), bfhi(xb[0]), bflo(xb[1]), bfhi(xb[1])};
      xv[t] = v;
      const float mu = (v[0] + v[1] + v[2] + v[3]) * 0.25f;
      const float q = (v[0] - mu) * (v[0] - mu) + (v[1] - mu) * (v[1] - mu) + (v[2] - mu) * (v[2] - mu) + (v[3] - mu) * (v[3] - mu);
      red[(mi * 16 + m16) * 37 + nf * 4 + g] = make_float2(mu, q);
    }
    lds_barrier();
    STAMP_AT(7);
    GAS u64* stg = (GAS u64*)(flags + FL_ST) + (size_t)rt * 128;
    if (wave == 0) {
      const int row = lane & 31, hf = lane >> 5;
      float2 e[18];
#pragma unroll
      for (int i = 0; i < 18; ++i) e[i] = red[row * 37 + hf * 18 + i];
      float mu = 0.f;
#pragma unroll
      for (int i = 0; i < 18; ++i) mu += e[i].x;
      mu += __shfl_xor(mu, 32);
      mu *= (1.f / 36.f);
      float q = 0.f;
#pragma unroll
      for (int i = 0; i < 18; ++i) q += e[i].y + 4.f * (e[i].x - mu) * (e[i].x - mu);
      q += __shfl_xor(q, 32);
      if (lane < 32) __hip_atomic_store(stg + nt * 32 + row, ((u64)(__float_as_uint(q) | 0x80000000u) << 32) | __float_as_uint(mu), RLX_AGENT);
      u64 e0v = 0, e1v = 0;
#pragma nounroll
      for (unsigned spin = 0; spin < SPIN_CAP; ++spin) {
        e0v = __hip_atomic_load(stg + lane, RLX_AGENT);
        e1v = __hip_atomic_load(stg + 64 + lane, RLX_AGENT);
        if (__all((e0v >> 63) && (e1v >> 63))) break;
        __builtin_amdgcn_s_sleep(1);
      }
      const float m0 = __uint_as_float((unsigned)e0v), m1 = __uint_as_float((unsigned)e1v);
      const float q0 = __uint_as_float((unsigned)(e0v >> 32) & 0x7fffffffu), q1 = __uint_as_float((unsigned)(e1v >> 32) & 0x7fffffffu);
      float ms = m0 + m1; ms += __shfl_xor(ms, 32);
      const float mean = 0.25f * ms;
      float qs = q0 + q1 + 144.f * ((m0 - mean) * (m0 - mean) + (m1 - mean) * (m1 - mean));
      qs += __shfl_xor(qs, 32);
      const float rstd = __builtin_amdgcn_rsqf(qs * (1.f / D_) + 1e-5f);
      if (lane < 32) ((float2*)(smem + RST))[row] = make_float2(rstd, -mean * rstd);
    }
    lds_barrier();
    STAMP_AT(8);
    const rsrc_t hrs2 = mk_rsrc(ws + WS_H2F, S_ * D_ * 2);
#pragma unroll
    for (int t = 0; t < 3; ++t) {
      if (t == 2 && wave >= 2) continue;
      const int nf = t < 2 ? wave : 8, mi = t < 2 ? t : (wave & 1);
      const float2 st = ((const float2*)(smem + RST))[mi * 16 + m16];
      const f32x4 sbv = *(const f32x4*)(mv + 144 + nf * 16 + 4 * g), cbv = *(const f32x4*)(mv + 288 + nf * 16 + 4 * g);
      float h[4];
#pragma unroll
      for (int j = 0; j < 4; ++j) h[j] = (xv[t][j] * st.x + st.y) * cbv[j] + sbv[j];
      const int nfg = nt * 9 + nf;
      u32x2 o = {pack_bf16(h[0], h[1]), pack_bf16(h[2], h[3])};
      st8_sc1(hrs2, (unsigned)((g * 32 + mi * 16 + m16) * 16), (unsigned)((rt * 18 + (nfg >> 1)) * 2048 + (nfg & 1) * 8), o);
    }
    wait_vm<0>();
    barrier_raw();
    if (tid == 0) set_flag(flags + FL_H2 + rt * 32 + nt, 1u);
    STAMP_AT(9);
  }

  bf16x8 xf[18][2];
  const int mf0 = (mt * 8 + wave) * 2;
  const char* xb = ws + WS_H2F + (size_t)(mt * 8 + wave) * 36 * 1024;
  const char* hb = ws + WS_HF + ((size_t)mf0 * 8 + xcd) * 18432;
  const unsigned* hfl = flags + FL_H + ((xcd * 8 + mt) * 8 + wave) * 16;
  const unsigned l16 = lane * 16;
  unsigned flagv;
  auto bload = [&](auto ac, auto mc) LAMBDA_INLINE {
    constexpr int a = decltype(ac)::value, mi = decltype(mc)::value;
    if constexpr (a < 18) {
      constexpr int o = (a * 2 + mi) * 1024;
      gload16_sc1<o % 4096>(xf[a][mi], l16, xb + (o / 4096) * 4096);
    } else {
      constexpr int o = (a - 18) * 1024;
      gload16_sc1<o % 4096>(xf[a - 18][mi], l16, hb + mi * 8 * 18432 + (o / 4096) * 4096);
    }
  };
  wait_flags(flags + FL_H2 + (mt * 8 + wave) * 32, 4, 1u, lane);
  STAMP_AT(10);
  static_for<SC.pro_n>([&](auto ic) LAMBDA_INLINE { constexpr int i = decltype(ic)::value; bload(IC(SC.pro_a[i]), IC(SC.pro_b[i])); });

  unsigned ubase[3][2];
  auto block_barrier = [&](auto Gc) LAMBDA_INLINE {
    constexpr int G = decltype(Gc)::value, u0 = SC.blk_u0[G], nu = SC.blk_nu[G], c = unit_chunk(u0);
    sched_fence();
    wait_vm<SC.bar_wait[G]>();
    barrier_raw();
    const int ln = lane_now(), p = ln & 3, q = (ln >> 2) & 3, blk = (ln >> 4) & 1, hh = ln >> 5;
#pragma unroll
    for (int k = 0; k < nu; ++k) {
      const unsigned sb = lds0 + slot_of(u0 + k) * SLOT + 8 * p;
      if constexpr (c == 0) ubase[k][0] = sb + (4 * hh + q) * 288 + blk * 128;
      else if constexpr (c == 1) ubase[k][0] = sb + (4 * hh + q) * 192 + blk * 96;
      else if constexpr (c == 2) {
        ubase[k][0] = sb + (4 * hh + q) * 128 + blk * 64 + 32 * (q >> 1);
        ubase[k][1] = sb + (4 * hh + q) * 128 + blk * 64 + 32 * (1 - (q >> 1));
      } else ubase[k][0] = sb + (16 * hh + 4 * blk + q) * 288;
      asm volatile("" : "+v"(ubase[k][0]));
      if constexpr (c == 2) asm volatile("" : "+v"(ubase[k][1]));
    }
    sched_fence();
  };
  auto afrag = [&](auto Gc) LAMBDA_INLINE {
    constexpr int G = decltype(Gc)::value, U = SC.unit[G], c = unit_chunk(U), k = SC.ub[G], kl = SC.kl[G], sb = SC.sub[G];
    if constexpr (c == 0) {
      constexpr unsigned o = (kl * 32 + 8 * sb) * 288 + SC.tile[G] * 32;
      return tr_frag(ubase[k][0] + o, ubase[k][0] + o + 16 * 288);
    } else if constexpr (c == 1) {
      constexpr unsigned o = (kl * 32 + 8 * sb) * 192 + (SC.tile[G] - 4) * 32;
      return tr_frag(ubase[k][0] + o, ubase[k][0] + o + 16 * 192);
    } else if constexpr (c == 2) {
      constexpr unsigned o = (kl * 32 + 8 * sb) * 128;
      return tr_frag(ubase[k][SC.tile[G] - 7] + o, ubase[k][SC.tile[G] - 7] + o + 16 * 128);
    } else {
      constexpr unsigned o = kl * 32 * 288 + SC.tile[G] * 32;
      return tr_frag(ubase[k][0] + o, ubase[k][0] + o + 8 * 288);
    }
  };
  f32x16 acc[4];
  f32x4 acc2[9][2];
  bf16x8 af[PF + 1];
  float ta[2], tp[2], hv[2];
  u32x4 pk;
  const rsrc_t hrs = mk_rsrc(ws + WS_HF, 128 * 8 * 18432);
  const rsrc_t prs = mk_rsrc(ws + WS_PART, 64 * 32 * 9 * 1024);
  const unsigned hso = (unsigned)((mf0 * 8 + xcd) * 18432);
  const unsigned tile = (unsigned)(mt * 4 + ftl);
  auto epi = [&](auto Gc) LAMBDA_INLINE {
    constexpr int G = decltype(Gc)::value;
    if constexpr (SC.eC[G] >= 0) {
      constexpr int e = SC.eC[G], i = e >> 3, j = e & 7;
      hv[j & 1] = tp[e & 1] * ta[e & 1];
      if constexpr (j & 1) {
        pk[j >> 1] = pack_bf16(hv[0], hv[1]);
        asm volatile("" : "+v"(pk));
      } else {
        asm volatile("" : "+v"(hv[0]));
      }
      if constexpr (j == 7) {
        const int ln = lane_now();
        const int pos = i < 4 ? 4 * ftl + i : i < 7 ? 16 + 3 * ftl + (i - 4) : 28 + 2 * ftl + (i - 7);
        st16_sc1(hrs, (unsigned)(((ln >> 4) & 1) * 8 * 18432 + (ln >> 5) * 256 + (ln & 15) * 16), hso + pos * 512, pk);
      }
    }
    if constexpr (SC.eB[G] >= 0) {
      constexpr int e = SC.eB[G];
      ta[e & 1] = __builtin_amdgcn_rcpf(1.f + ta[e & 1]);
      asm volatile("" : "+v"(ta[e & 1]));
    }
    if constexpr (SC.eA[G] >= 0) {
      constexpr int e = SC.eA[G], i = e >> 3, j = e & 7, st = tile_set(i);
      const float gv = acc[st][j];
      tp[e & 1] = gv * acc[st][8 + j];
      ta[e & 1] = __builtin_amdgcn_exp2f(gv * -1.44269504089f);
      asm volatile("" : "+v"(ta[e & 1]), "+v"(tp[e & 1]));
    }
  };
  auto part_store = [&](auto nc) LAMBDA_INLINE {
    constexpr int nf = decltype(nc)::value;
    u32x4 v = {pack_bf16(acc2[nf][0][0], acc2[nf][0][1]), pack_bf16(acc2[nf][0][2], acc2[nf][0][3]),
               pack_bf16(acc2[nf][1][0], acc2[nf][1][1]), pack_bf16(acc2[nf][1][2], acc2[nf][1][3])};
    if (wave == xcd) *(u32x4*)(smem + OWN + nf * 1024 + l16) = v;
    else st16_sc1(prs, l16, (((wave * 8 + xcd) * 32 + tile) * 9 + nf) * 1024, v);
  };

  block_barrier(IC(0));
  static_for<PF>([&](auto ic) LAMBDA_INLINE { constexpr int G = decltype(ic)::value; af[G % (PF + 1)] = afrag(IC(G)); });
  static_for<NS>([&](auto ic) LAMBDA_INLINE {
    constexpr int G = decltype(ic)::value, GN = G + PF, kk = SC.bi[G];
    if constexpr (G == NS1) STAMP_AT(11);
    if constexpr (G == 1) STAMP_AT(16);
    if constexpr (GN < NS) {
      if constexpr (SC.blk_u0[GN] >= 0) block_barrier(IC(GN));
    }
    if constexpr (SC.op_t[G] == OP_W) stage_round(IC(SC.op_a[G]), IC(SC.op_b[G]));
    if constexpr (SC.op_t[G] == OP_B) bload(IC(SC.op_a[G]), IC(SC.op_b[G]));
    if constexpr (SC.op_t[G] == OP_POLL) gload4_sc1(flagv, (lane_now() & 3) * 4, hfl);
    if constexpr (SC.op_t[G] == OP_PF) {
      int pc = SC.op_a[G] * 8 + wave; if (pc > 19) pc = 19;
      glds16(l16, (const char*)w2 + (size_t)(xcd * 576 + idx * 18) * 1152 + pc * 1024 + (pc == 19 && wave > 3 ? 256 : 0), lds0 + SCRATCH);
    }
    if constexpr (SC.chk[G] != 0) {
      asm volatile("s_waitcnt vmcnt(%1)" : "+v"(flagv) : "i"(SC.chk_wait[G]) : "memory");
      if (!__all(flagv >= (unsigned)SC.chk[G])) wait_flags(hfl, 4, (unsigned)SC.chk[G], lane_now());
    }
    if constexpr (SC.bwait[G] >= 0)
      asm volatile("s_waitcnt vmcnt(%2)" : "+v"(xf[kk][0]), "+v"(xf[kk][1]) : "i"(SC.bwait[G]) : "memory");
    if constexpr (G < NS1) {
      constexpr int st = tile_set(SC.tile[G]);
      const f32x16 z = {};
      acc[st] = __builtin_amdgcn_mfma_f32_32x32x16_bf16(af[G % (PF + 1)], xf[kk][SC.sub[G]], SC.first[G] ? z : acc[st], 0, 0, 0);
      af[GN % (PF + 1)] = afrag(IC(GN));
    } else {
      constexpr int nf = SC.tile[G];
      const f32x4 z = {};
      acc2[nf][0] = __builtin_amdgcn_mfma_f32_16x16x32_bf16(af[G % (PF + 1)], xf[kk][0], SC.first[G] ? z : acc2[nf][0], 0, 0, 0);
      if constexpr (GN < NS) af[GN % (PF + 1)] = afrag(IC(GN));
      acc2[nf][1] = __builtin_amdgcn_mfma_f32_16x16x32_bf16(af[G % (PF + 1)], xf[kk][1], SC.first[G] ? z : acc2[nf][1], 0, 0, 0);
    }
    epi(IC(G));
    if constexpr (SC.pst[G] >= 0) part_store(IC(SC.pst[G]));
    if constexpr (SC.pub[G] != 0) {
      wait_vm<SC.pub_wait[G]>();
      if (lane_now() == 0) set_flag((unsigned*)hfl + ftl, (unsigned)SC.pub[G]);
    }
    sched_fence();
  });
  STAMP_AT(12);

  {
    part_store(IC(8));
    const int ln = lane_now(), gg = ln >> 4, mm = ln & 15;
    wait_vm<0>();
    if (ln == 0 && wave != xcd) set_flag(flags + FL_P + (tile * 8 + wave) * 16 + xcd, 1u);
    STAMP_AT(13);
    lds_barrier();
    u32x4 pv[8]; u32x2 pe[8];
    const GAS unsigned* fp = (const GAS unsigned*)(flags + FL_P + (tile * 8 + xcd) * 16) + (ln & 7);
    unsigned pend = 0xffu & ~(1u << xcd);
#pragma unroll
    for (int s = 0; s < 8; ++s) {
      pv[s] = *(const u32x4*)(smem + OWN + wave * 1024 + ln * 16);
      pe[s] = *(const u32x2*)(smem + OWN + 8 * 1024 + ln * 16 + (wave & 1) * 8);
    }
#pragma nounroll
    for (unsigned spin = 0; spin < SPIN_CAP && pend; ++spin) {
      const unsigned ready = (unsigned)__builtin_amdgcn_readfirstlane((unsigned)__ballot(__hip_atomic_load(fp, RLX_AGENT) != 0u)) & pend;
      mem_fence();
#pragma unroll
      for (int s = 0; s < 8; ++s) {
        if ((ready >> s) & 1u) {
          pv[s] = ld16_sc1(prs, ln * 16, (((xcd * 8 + s) * 32 + tile) * 9 + wave) * 1024);
          if (wave < 2) pe[s] = ld8_sc1(prs, ln * 16 + wave * 8, (((xcd * 8 + s) * 32 + tile) * 9 + 8) * 1024);
        }
      }
      pend &= ~ready;
      if (!ready) __builtin_amdgcn_s_sleep(1);
    }
    STAMP_AT(14);
    float y[3][4] = {};
#pragma unroll
    for (int s = 0; s < 8; ++s) {
#pragma unroll
      for (int t = 0; t < 2; ++t) {
        y[t][0] += bflo(pv[s][2 * t]); y[t][1] += bfhi(pv[s][2 * t]); y[t][2] += bflo(pv[s][2 * t + 1]); y[t][3] += bfhi(pv[s][2 * t + 1]);
      }
      if (wave < 2) { y[2][0] += bflo(pe[s][0]); y[2][1] += bfhi(pe[s][0]); y[2][2] += bflo(pe[s][1]); y[2][3] += bfhi(pe[s][1]); }
    }
#pragma unroll
    for (int t = 0; t < 3; ++t) {
      if (t == 2 && wave >= 2) continue;
      const int nf = t < 2 ? wave : 8, mi = t < 2 ? t : (wave & 1);
      const u32x2 x1 = *(const u32x2*)(smem + p2::X1S + ((nf * 2 + mi) * 64 + ln) * 8);
      const f32x4 gv = *(const f32x4*)(smem + p2::MV + (432 + nf * 16 + 4 * gg) * 4);
      u32x2 o = {pack_bf16(bflo(x1[0]) + gv[0] * y[t][0], bfhi(x1[0]) + gv[1] * y[t][1]),
                 pack_bf16(bflo(x1[1]) + gv[2] * y[t][2], bfhi(x1[1]) + gv[3] * y[t][3])};
      *(u32x2*)(out + (size_t)((mt * 8 + xcd) * 32 + mi * 16 + mm) * D_ + ftl * 144 + nf * 16 + 4 * gg) = o;
    }
  }
  STAMP_AT(15);
}

extern "C" __global__ void k_tail() {}

extern "C" void kernel_launch(void* const* d_in, const int* in_sizes, int n_in, void* d_out, int out_size, void* d_ws,
                              size_t ws_size, hipStream_t stream) {
  const u16* x = (const u16*)d_in[0];
  const u16* wqkv = (const u16*)d_in[1];
  const u16* wo = (const u16*)d_in[2];
  const u16* w1 = (const u16*)d_in[4];
  const u16* w3 = (const u16*)d_in[5];
  const u16* w2 = (const u16*)d_in[6];
  const u16* c = (const u16*)d_in[7];
  const u16* wmod = (const u16*)d_in[8];
  (void)hipFuncSetAttribute((const void*)k_main, hipFuncAttributeMaxDynamicSharedMemorySize, LDS_BYTES);
  k_pre<<<k0::NB, 512, 0, stream>>>(x, wqkv, wo, c, wmod, (char*)d_ws);
  k_main<<<256, 512, LDS_BYTES, stream>>>(x, w1, w3, w2, (char*)d_ws, (u16*)d_out);
}
```

```cpp
#include <hip/hip_runtime.h>
#include <hip/hip_bf16.h>
#include <stdint.h>
#include <utility>

typedef unsigned short u16;
typedef unsigned long long u64;
typedef short bf16x8 __attribute__((ext_vector_type(8)));
typedef short s16x4 __attribute__((ext_vector_type(4)));
typedef float f32x4 __attribute__((ext_vector_type(4)));
typedef float f32x16 __attribute__((ext_vector_type(16)));
typedef unsigned u32x4 __attribute__((ext_vector_type(4)));
typedef unsigned u32x2 __attribute__((ext_vector_type(2)));
#define LAS __attribute__((address_space(3)))
#define GAS __attribute__((address_space(1)))
#define DI __device__ __forceinline__
#define LAMBDA_INLINE __attribute__((always_inline))
#define IC(x) std::integral_constant<int, (x)>{}
#define RLX_AGENT __ATOMIC_RELAXED, __HIP_MEMORY_SCOPE_AGENT

constexpr int S_ = 2048, D_ = 576, F_ = 4608;

#define STAMP_AT(i) do {} while (0)

template <int... Is, class Fn> DI void static_for_impl(std::integer_sequence<int, Is...>, Fn&& f) { (f(std::integral_constant<int, Is>{}), ...); }
template <int N, class Fn> DI void static_for(Fn&& f) { static_for_impl(std::make_integer_sequence<int, N>{}, f); }

DI float bf2f(u16 v) { return __uint_as_float(((unsigned)v) << 16); }
DI float bflo(unsigned v) { return __uint_as_float(v << 16); }
DI float bfhi(unsigned v) { return __uint_as_float(v & 0xffff0000u); }
DI unsigned pack_bf16(float lo, float hi) {
  typedef __bf16 bf2 __attribute__((ext_vector_type(2)));
  typedef float f2 __attribute__((ext_vector_type(2)));
  f2 v = {lo, hi};
  bf2 r = __builtin_convertvector(v, bf2);
  return __builtin_bit_cast(unsigned, r);
}
DI float rbf(float f) { return __uint_as_float(pack_bf16(f, 0.f) << 16); }
DI float silu_f(float g) { return g * __builtin_amdgcn_rcpf(1.f + __builtin_amdgcn_exp2f(g * -1.44269504089f)); }

template <int N> DI void wait_vm() { asm volatile("s_waitcnt vmcnt(%0)" ::"i"(N) : "memory"); }
DI void barrier_raw() { asm volatile("" ::: "memory"); __builtin_amdgcn_s_barrier(); asm volatile("" ::: "memory"); }
DI void lds_barrier() { asm volatile("s_waitcnt lgkmcnt(0)" ::: "memory"); __builtin_amdgcn_s_barrier(); asm volatile("" ::: "memory"); }
DI void sched_fence() { __builtin_amdgcn_sched_barrier(0); }
DI int lane_now() {
  int l = __builtin_amdgcn_mbcnt_hi(~0u, __builtin_amdgcn_mbcnt_lo(~0u, 0u));
  asm volatile("" : "+v"(l));
  return l;
}

DI void glds16(unsigned voff, const void* sbase, unsigned lds_dst) {
  unsigned keep;
  asm volatile("s_mov_b32 %0, m0\n\ts_mov_b32 m0, %3\n\ts_nop 0\n\tglobal_load_lds_dwordx4 %1, %2\n\ts_mov_b32 m0, %0"
               : "=&s"(keep) : "v"(voff), "s"(sbase), "s"(lds_dst) : "memory");
}
DI void glds16v(const void* vaddr, unsigned lds_dst) {
  unsigned keep;
  asm volatile("s_mov_b32 %0, m0\n\ts_mov_b32 m0, %2\n\ts_nop 0\n\tglobal_load_lds_dwordx4 %1, off\n\ts_mov_b32 m0, %0"
               : "=&s"(keep) : "v"(vaddr), "s"(lds_dst) : "memory");
}
template <int IMM> DI void gload16_sc1(bf16x8& dst, unsigned voff, const void* sbase) {
  asm volatile("global_load_dwordx4 %0, %1, %2 offset:%3 sc1" : "=v"(dst) : "v"(voff), "s"(sbase), "i"(IMM) : "memory");
}
DI void gload8_sc1(u32x2& dst, unsigned voff, const void* sbase) {
  asm volatile("global_load_dwordx2 %0, %1, %2 sc1" : "=v"(dst) : "v"(voff), "s"(sbase) : "memory");
}
DI void gload4_sc1(unsigned& dst, unsigned voff, const void* sbase) {
  asm volatile("global_load_dword %0, %1, %2 sc1" : "=v"(dst) : "v"(voff), "s"(sbase) : "memory");
}
DI s16x4 tr_read(unsigned addr) { return __builtin_amdgcn_ds_read_tr16_b64_v4i16((LAS s16x4*)(uintptr_t)addr); }
DI bf16x8 tr_frag(unsigned a0, unsigned a1) {
  s16x4 lo = tr_read(a0), hi = tr_read(a1);
  return __builtin_shufflevector(lo, hi, 0, 1, 2, 3, 4, 5, 6, 7);
}
typedef __amdgpu_buffer_rsrc_t rsrc_t;
DI rsrc_t mk_rsrc(const void* p, unsigned bytes) { return __builtin_amdgcn_make_buffer_rsrc((void*)p, 0, bytes, 0x00020000); }
DI void st16_sc1(rsrc_t r, unsigned voff, unsigned soff, u32x4 v) {
  __builtin_amdgcn_raw_buffer_store_b128(v, r, voff, soff, 16);
  asm volatile("s_nop 1" ::"v"(v) : "memory");
}
DI void st8_sc1(rsrc_t r, unsigned voff, unsigned soff, u32x2 v) { __builtin_amdgcn_raw_buffer_store_b64(v, r, voff, soff, 16); }
DI u32x4 ld16(rsrc_t r, unsigned voff, unsigned soff) { return __builtin_amdgcn_raw_buffer_load_b128(r, voff, soff, 0); }
DI u32x2 ld8(rsrc_t r, unsigned voff, unsigned soff) { return __builtin_amdgcn_raw_buffer_load_b64(r, voff, soff, 0); }
DI bf16x8 ldfrag(rsrc_t r, unsigned voff, unsigned soff) { return __builtin_bit_cast(bf16x8, ld16(r, voff, soff)); }
DI f32x4 ldf4(rsrc_t r, unsigned voff, unsigned soff) { return __builtin_bit_cast(f32x4, ld16(r, voff, soff)); }
DI u32x4 ld16_sc1(rsrc_t r, unsigned voff, unsigned soff) { return __builtin_amdgcn_raw_buffer_load_b128(r, voff, soff, 16); }
DI u32x2 ld8_sc1(rsrc_t r, unsigned voff, unsigned soff) { return __builtin_amdgcn_raw_buffer_load_b64(r, voff, soff, 16); }

constexpr unsigned SPIN_CAP = 1u << 15;
DI void wait_flags(const unsigned* f, int n, unsigned need, int lane) {
  const GAS unsigned* p = (const GAS unsigned*)f + (lane < n ? lane : 0);
#pragma nounroll
  for (unsigned spin = 0; spin < SPIN_CAP; ++spin) {
    const unsigned v = __hip_atomic_load(p, RLX_AGENT);
    if (__all(v >= need)) break;
    __builtin_amdgcn_s_sleep(1);
  }
  asm volatile("" ::: "memory");
}
DI void set_flag(unsigned* f, unsigned v) { __hip_atomic_store((GAS unsigned*)f, v, RLX_AGENT); }

constexpr size_t WS_MODF = 0;
constexpr size_t WS_MODB = 65536;
constexpr size_t WS_N1 = 60u << 20;
constexpr size_t WS_FLAGS = 131072;
constexpr size_t WS_OF = 1u << 20;
constexpr size_t WS_H2F = 4u << 20;
constexpr size_t WS_WQF = 8u << 20;
constexpr size_t WS_WOF = 12u << 20;
constexpr size_t WS_HF = 16u << 20;
constexpr size_t WS_PART = 40u << 20;
constexpr int FL_O = 0;
constexpr int FL_H2 = 2048;
constexpr int FL_H = 4096;
constexpr int FL_P = FL_H + 8192;
constexpr int FL_ST = FL_P + 4096;
constexpr int FLAG_WORDS = FL_ST + 16384;

#define PACK_AUX 0
namespace k0 {
constexpr int B_PQ = 112, B_STAT = 184, B_PO = 216, B_CODE = 240, B_FLAG = 248, NB = 256;
}
DI void k_pre_body(const u16* __restrict__ x, const u16* __restrict__ wqkv, const u16* __restrict__ wo,
                   const u16* __restrict__ cvec, const u16* __restrict__ wmod, char* __restrict__ ws) {
  using namespace k0;
  __shared__ __attribute__((aligned(16))) char sm[8 * 32 * 160];
  const int tid = threadIdx.x, lane = tid & 63, wave = __builtin_amdgcn_readfirstlane(tid >> 6);
  const int b = blockIdx.x;
  if ((b >= B_PQ && b < B_STAT) || (b >= B_PO && b < B_CODE)) {
    const bool isq = b < B_STAT;
    const int t = isq ? ((b - B_PQ) >> 3) * 8 + wave : (b - B_PO) * 8 + wave;
    if (!isq && t >= 162) return;
    const int kap = t % 18, grp = t / 18, hg = b & 7;
    char* img = sm + wave * (32 * 160);
    const int ch = lane & 7;
    const u16* src;
    if (isq) {
      int c8 = grp * 8 + ch; if (c8 > 26) c8 = 26;
      src = wqkv + (size_t)(kap * 32 + (lane >> 3)) * 1728 + (c8 / 9) * 576 + hg * 72 + (c8 % 9) * 8;
    } else {
      src = wo + (size_t)(kap * 32 + (lane >> 3)) * D_ + grp * 64 + ch * 8;
    }
    const int pitch8 = isq ? 8 * 1728 : 8 * D_;
    u32x4 v[4];
#pragma unroll
    for (int i = 0; i < 4; ++i) v[i] = *(const u32x4*)(src + i * pitch8);
#pragma unroll
    for (int i = 0; i < 4; ++i) *(u32x4*)(img + (i * 8 + (lane >> 3)) * 160 + ch * 16) = v[i];
    asm volatile("s_waitcnt lgkmcnt(0)" ::: "memory");
    const unsigned a0 = (unsigned)(uintptr_t)(LAS char*)img + (8 * (lane >> 4) + ((lane & 15) >> 2)) * 160 + 8 * (lane & 3);
    const rsrc_t dr = mk_rsrc(isq ? ws + WS_WQF + (size_t)(hg * 14 * 18) * 1024 : ws + WS_WOF, 36 * 18 * 1024);
#pragma unroll
    for (int f = 0; f < 4; ++f) {
      const bf16x8 fr = tr_frag(a0 + f * 32, a0 + f * 32 + 4 * 160);
      const int nf = grp * 4 + f;
      if (!isq || nf < 14) __builtin_amdgcn_raw_buffer_store_b128(__builtin_bit_cast(u32x4, fr), dr, (nf * 18 + kap) * 1024 + lane * 16, 0, PACK_AUX);
    }
    return;
  }
  if (b < B_PQ) {
    const int j = b, cg = ((j >> 4) * 8 + (j & 7)) * 2 + ((j >> 3) & 1);
    if (cg >= 108) return;
    const int rg = tid >> 2, cl = tid & 3;
    const rsrc_t wr = mk_rsrc(wmod, 576 * 3456 * 2), cr = mk_rsrc(cvec, 576 * 2);
    u32x4 w[5]; unsigned cv[5];
#pragma unroll
    for (int i = 0; i < 5; ++i) {
      w[i] = ld16(wr, (unsigned)(rg * 3456 + cl * 8) * 2u, (unsigned)(i * 128 * 3456 + cg * 32) * 2u);
      cv[i] = __builtin_amdgcn_raw_buffer_load_b16(cr, rg * 2, i * 256, 0);
    }
    float acc[8] = {};
#pragma unroll
    for (int i = 0; i < 5; ++i) {
      const float s = rbf(silu_f(bflo(cv[i])));
#pragma unroll
      for (int q = 0; q < 4; ++q) { acc[2 * q] += s * bflo(w[i][q]); acc[2 * q + 1] += s * bfhi(w[i][q]); }
    }
#pragma unroll
    for (int q = 0; q < 8; ++q) {
      acc[q] += __int_as_float(__builtin_amdgcn_update_dpp(0, __float_as_int(acc[q]), 0x124, 0xf, 0xf, false));
      acc[q] += __int_as_float(__builtin_amdgcn_update_dpp(0, __float_as_int(acc[q]), 0x128, 0xf, 0xf, false));
    }
    float* red = (float*)sm;
    if ((lane & 15) < 4) {
      *(f32x4*)(red + (tid >> 4) * 36 + cl * 8) = (f32x4){acc[0], acc[1], acc[2], acc[3]};
      *(f32x4*)(red + (tid >> 4) * 36 + cl * 8 + 4) = (f32x4){acc[4], acc[5], acc[6], acc[7]};
    }
    __syncthreads();
    if (tid < 32) {
      float v = 0.f;
#pragma unroll
      for (int q = 0; q < 32; ++q) v += red[q * 36 + tid];
      v = rbf(v);
      const int vec = cg / 18;
      if (vec == 1 || vec == 4) v = rbf(1.f + v);
      ((float*)(ws + WS_MODF))[cg * 32 + tid] = v;
      if (vec < 2) ((u16*)(ws + WS_MODB))[((cg - vec * 18) * 32 + tid) * 2 + vec] = (u16)(__float_as_uint(v) >> 16);
    }
    return;
  }
  if (b < B_PO) {
    const int row = (b & 7) * 256 + (((b - B_STAT) >> 3) * 8 + wave) * 8 + (lane >> 3), cl = lane & 7;
    u32x4 v[9];
#pragma unroll
    for (int i = 0; i < 9; ++i) v[i] = *(const u32x4*)(x + (size_t)row * D_ + (cl + 8 * i) * 8);
    float s = 0.f;
#pragma unroll
    for (int i = 0; i < 9; ++i)
#pragma unroll
      for (int j = 0; j < 4; ++j) s += bflo(v[i][j]) + bfhi(v[i][j]);
    s += __shfl_xor(s, 1); s += __shfl_xor(s, 2); s += __shfl_xor(s, 4);
    const float mean = s * (1.f / D_);
    float q = 0.f;
#pragma unroll
    for (int i = 0; i < 9; ++i)
#pragma unroll
      for (int j = 0; j < 4; ++j) { const float a = bflo(v[i][j]) - mean, c = bfhi(v[i][j]) - mean; q += a * a + c * c; }
    q += __shfl_xor(q, 1); q += __shfl_xor(q, 2); q += __shfl_xor(q, 4);
    const float rstd = __builtin_amdgcn_rsqf(q * (1.f / D_) + 1e-5f);
    const float nm = -mean * rstd;
#pragma unroll
    for (int i = 0; i < 9; ++i) {
      u32x4 o;
#pragma unroll
      for (int j = 0; j < 4; ++j) o[j] = pack_bf16(bflo(v[i][j]) * rstd + nm, bfhi(v[i][j]) * rstd + nm);
      *(u32x4*)(ws + WS_N1 + ((size_t)row * D_ + (cl + 8 * i) * 8) * 2) = o;
    }
    return;
  }
  if (b < B_FLAG) {
    unsigned clo, chi, elo, ehi;
    asm volatile("s_getpc_b64 s[20:21]\n\ts_add_u32 %0, s20, k_main@rel32@lo+4\n\ts_addc_u32 %1, s21, k_main@rel32@hi+12"
                 : "=s"(clo), "=s"(chi)::"s20", "s21", "scc");
    asm volatile("s_getpc_b64 s[20:21]\n\ts_add_u32 %0, s20, k_tail@rel32@lo+4\n\ts_addc_u32 %1, s21, k_tail@rel32@hi+12"
                 : "=s"(elo), "=s"(ehi)::"s20", "s21", "scc");
    const GAS char* code = (const GAS char*)(((u64)chi << 32) | clo);
    const long long len = (long long)(((u64)ehi << 32) | elo) - (long long)(((u64)chi << 32) | clo);
    unsigned acc = 0;
#pragma unroll
    for (int i = 0; i < 4; ++i) {
      const int o = (i * 512 + tid) * 64;
      if (o + 4 <= len) acc += *(const GAS unsigned*)(code + o);
    }
    asm volatile("" ::"v"(acc));
    return;
  }
  u32x4* fl = (u32x4*)(ws + WS_FLAGS);
  for (int i = (b - B_FLAG) * 512 + tid; i < FLAG_WORDS / 4; i += 8 * 512) fl[i] = (u32x4){0u, 0u, 0u, 0u};
}

__global__ __launch_bounds__(512) void k_pre(const u16* __restrict__ x, const u16* __restrict__ wqkv, const u16* __restrict__ wo,
                                             const u16* __restrict__ cvec, const u16* __restrict__ wmod, char* __restrict__ ws) {
  k_pre_body(x, wqkv, wo, cvec, wmod, ws);
}

#define NPRE_ 5
namespace mlp {
constexpr int NBUF = 8, SLOT = 18432, PF = 3, NPRE = NPRE_;
constexpr int RING_BYTES = NBUF * SLOT;
constexpr int OWN = 1 * SLOT;
constexpr int NU1 = 21, NU2 = 9, NU = NU1 + NU2;
constexpr int NS1 = 324, NS2 = 162, NS = NS1 + NS2;
enum { OP_NONE = 0, OP_W, OP_B, OP_POLL, OP_PF };
#define TA_COST 28
constexpr int CHK_DELAY = 16;
constexpr int slot_of(int U) { int u = U % NBUF; return u < 3 ? 5 + u : u < 5 ? u : u - 5; }
constexpr int unit_chunk(int U) { return U < 9 ? 0 : U < 15 ? 1 : U < 21 ? 2 : 3; }
constexpr int unit_np(int U) { return unit_chunk(U) == 2 ? 12 : 18; }
constexpr int unit_nk(int U) { int c = unit_chunk(U); return (c == 0 || c == 3) ? 2 : 3; }
constexpr int unit_k0(int U) { return U < 9 ? 2 * U : U < 15 ? 3 * (U - 9) : U < 21 ? 3 * (U - 15) : 2 * (U - 21); }
constexpr int chunk_nt(int c) { return c == 0 ? 4 : c == 1 ? 3 : 2; }
constexpr int chunk_t0(int c) { return c == 0 ? 0 : c == 1 ? 4 : 7; }
constexpr int tile_set(int i) { return i < 4 ? i : i < 7 ? i - 4 : i == 7 ? 3 : 0; }
constexpr int run_a0(int l) { return l == 0 ? 0 : l == 1 ? 3 : l == 2 ? 2 : 5; }
constexpr int run_c1(int l) { return l == 0 ? 4 : l == 1 ? 0 : l == 2 ? 6 : 0; }
constexpr int run_e0(int l) { return l == 0 ? 7 : l == 1 ? 7 : l == 2 ? 0 : 3; }
constexpr int pos_row(int pos) {
  if (pos < 16) return (pos >> 2) * 144 + 16 * (run_a0(pos >> 2) + (pos & 3));
  if (pos < 28) return ((pos - 16) / 3) * 144 + 16 * (run_c1((pos - 16) / 3) + (pos - 16) % 3);
  return ((pos - 28) >> 1) * 144 + 16 * (run_e0((pos - 28) >> 1) + ((pos - 28) & 1));
}
struct Sched {
  short unit[NS], ub[NS], kl[NS], bi[NS], sub[NS], tile[NS], first[NS];
  short blk_u0[NS], blk_nu[NS], bar_wait[NS];
  short op_t[NS], op_a[NS], op_b[NS];
  short pro_n, pro_a[16], pro_b[16];
  short bwait[NS];
  short eA[NS], eB[NS], eC[NS];
  short pub[NS], pub_wait[NS], chk[NS], chk_wait[NS];
  short pst[NS];
  bool ok;
  short min_lead_b, min_lead_w;
};
constexpr Sched make_sched() {
  Sched s{};
  s.ok = true; s.min_lead_b = 1000; s.min_lead_w = 1000;
  int G = 0;
  int blocks[26][2] = {{0,1},{1,1},{2,1},{3,1},{4,1},{5,1},{6,1},{7,2},{9,2},{11,1},{12,1},{13,2},{15,2},{17,1},{18,1},{19,2},
                       {21,1},{22,1},{23,1},{24,1},{25,1},{26,1},{27,3},{-1,0}};
  int blk_of_unit[NU] = {};
  for (int i = 0; i < NS; ++i) { s.blk_u0[i] = -1; s.bar_wait[i] = -1; s.bwait[i] = -1; s.eA[i] = -1; s.eB[i] = -1; s.eC[i] = -1; s.pst[i] = -1; }
  for (int B = 0; blocks[B][0] >= 0; ++B) {
    int u0 = blocks[B][0], nu = blocks[B][1], c = unit_chunk(u0);
    s.blk_u0[G] = (short)u0; s.blk_nu[G] = (short)nu;
    for (int k = 0; k < nu; ++k) blk_of_unit[u0 + k] = B;
    if (c < 3) {
      for (int t = 0; t < chunk_nt(c); ++t)
        for (int k = 0; k < nu; ++k)
          for (int kl = 0; kl < unit_nk(u0); ++kl)
            for (int m = 0; m < 2; ++m) {
              s.unit[G] = (short)(u0 + k); s.ub[G] = (short)k; s.kl[G] = (short)kl; s.bi[G] = (short)(unit_k0(u0 + k) + kl);
              s.sub[G] = (short)m; s.tile[G] = (short)(chunk_t0(c) + t); s.first[G] = (short)(s.bi[G] == 0 && m == 0); ++G;
            }
    } else if (nu == 1) {
      for (int kl = 0; kl < 2; ++kl)
        for (int nf = 0; nf < 9; ++nf) {
          s.unit[G] = (short)u0; s.ub[G] = 0; s.kl[G] = (short)kl; s.bi[G] = (short)(unit_k0(u0) + kl);
          s.tile[G] = (short)nf; s.first[G] = (short)(s.bi[G] == 0); ++G;
        }
    } else {
      for (int nf = 0; nf < 9; ++nf)
        for (int k = 0; k < nu; ++k)
          for (int kl = 0; kl < 2; ++kl) {
            s.unit[G] = (short)(u0 + k); s.ub[G] = (short)k; s.kl[G] = (short)kl; s.bi[G] = (short)(unit_k0(u0 + k) + kl);
            s.tile[G] = (short)nf; ++G;
            if (nf > 0 && k == 0 && kl == 1) s.pst[G - 1] = (short)(nf - 1);
          }
    }
  }
  int store_at[NS] = {}, last_store[3] = {};
  int prev = 0;
  for (int i = 0; i < 9; ++i) {
    int R = 0, D = 100000;
    for (int g = 0; g < NS1; ++g) if (s.tile[g] == i) R = g + 1;
    for (int g = R; g < NS1; ++g) if (tile_set(s.tile[g]) == tile_set(i)) { D = g; break; }
    int st = R > prev ? R : prev;
    int sp = (st + 14 < D) ? 2 : 1;
    if (st + sp * 7 >= D) s.ok = false;
    for (int j = 0; j < 8; ++j) {
      int g = st + sp * j;
      s.eA[g] = (short)(i * 8 + j); s.eB[g + 1] = (short)(i * 8 + j); s.eC[g + 2] = (short)(i * 8 + j);
      if (j == 7) { store_at[g + 2] += 1; last_store[i < 4 ? 0 : i < 7 ? 1 : 2] = g + 2; }
    }
    prev = st + sp * 8;
  }
  const int poll_at[3] = {236, 326, 388};
  for (int c = 0; c < 3; ++c) s.pub[last_store[c] + (c == 2 ? 10 : 14)] = (short)(c + 1);
  int xlast[18] = {};
  for (int g = 0; g < NS1; ++g) xlast[s.bi[g]] = g;
  int ba[96] = {}, bb[96] = {}, be[96] = {}, nb = 0;
  for (int k = 0; k < 18; ++k) for (int m = 0; m < 2; ++m) { ba[nb] = k; bb[nb] = m; be[nb] = 0; ++nb; }
  for (int c = 0; c < 3; ++c) {
    int k0 = c == 0 ? 0 : c == 1 ? 8 : 14, k1 = c == 0 ? 8 : c == 1 ? 14 : 18;
    for (int k = k0; k < k1; ++k) for (int m = 0; m < 2; ++m) {
      int e = poll_at[c] + CHK_DELAY + 1; if (xlast[k] + 1 > e) e = xlast[k] + 1;
      ba[nb] = 18 + k; bb[nb] = m; be[nb] = e; ++nb;
    }
  }
  int wa[96] = {}, wb[96] = {}, nw = 0;
  for (int U = NPRE; U < NU; ++U) for (int j = 0; j < (unit_np(U) + 7) / 8; ++j) { wa[nw] = U; wb[nw] = j; ++nw; }
  int bi = 0, wi = 0;
  while (bi < 8) { s.pro_a[s.pro_n] = (short)ba[bi]; s.pro_b[s.pro_n] = (short)bb[bi]; ++s.pro_n; ++bi; }
  int wneed[NU] = {}, bneed[36] = {};
  for (int g = NS - 1; g >= 0; --g) {
    bneed[(g >= NS1 ? 18 : 0) + s.bi[g]] = g;
    if (s.blk_u0[g] >= 0) for (int k = 0; k < s.blk_nu[g]; ++k) wneed[s.blk_u0[g] + k] = g - PF;
  }
  {
    int bars_done = 0, ta_free = 0;
    for (int g = 0; g < NS; ++g) {
      int gn = g + PF;
      if (gn < NS && s.blk_u0[gn] >= 0) bars_done = blk_of_unit[s.blk_u0[gn]];
      bool polled = false;
      for (int c = 0; c < 3; ++c) if (poll_at[c] == g) { s.op_t[g] = OP_POLL; s.op_a[g] = (short)c; s.chk[g + CHK_DELAY] = (short)(c + 1); polled = true; }
      for (int c = 0; c < 3; ++c) if (g == 151 + 3 * c) { s.op_t[g] = OP_PF; s.op_a[g] = (short)c; polled = true; }
      if (polled || ta_free > g * 10 + 5) continue;
      const int base = ta_free > g * 10 - 5 ? ta_free : g * 10 - 5;
      bool wready = wi < nw && !(wa[wi] - NBUF >= 0 && blk_of_unit[wa[wi] - NBUF] > bars_done - 2);
      bool bready = bi < nb && g >= be[bi];
      int bdl = bready ? bneed[ba[bi]] : 1000000;
      int wdl = wready ? wneed[wa[wi]] - 12 : 1000000;
      if (bready && bdl <= wdl) {
        s.op_t[g] = OP_B; s.op_a[g] = (short)ba[bi]; s.op_b[g] = (short)bb[bi];
        ta_free = base + 8 * TA_COST / 10;
        if (bneed[ba[bi]] - g < s.min_lead_b) s.min_lead_b = (short)(bneed[ba[bi]] - g);
        ++bi;
      } else if (wready) {
        s.op_t[g] = OP_W; s.op_a[g] = (short)wa[wi]; s.op_b[g] = (short)wb[wi];
        const int c = unit_chunk(wa[wi]);
        int pieces = unit_np(wa[wi]) - 8 * wb[wi]; if (pieces > 8) pieces = 8;
        ta_free = base + (c == 0 ? 8 : c == 3 ? 11 : c == 1 ? 14 : 20) * TA_COST * pieces / 80;
        if (wneed[wa[wi]] - g < s.min_lead_w) s.min_lead_w = (short)(wneed[wa[wi]] - g);
        ++wi;
      }
    }
    if (bi < nb || wi < nw || s.min_lead_b <= 2 || s.min_lead_w <= 2) s.ok = false;
  }
  {
    int n = 0, wpos[NU] = {}, bpos[36] = {}, pollpos = 0, storepos = 0;
    bool bseen[36] = {};
    for (int i = 0; i < s.pro_n; ++i) { ++n; bpos[s.pro_a[i]] = n; }
    s.bar_wait[0] = (short)n;
    for (int g = 0; g < NS; ++g) {
      int gn = g + PF;
      if (gn < NS && s.blk_u0[gn] >= 0) {
        int need = 0;
        for (int k = 0; k < s.blk_nu[gn]; ++k) if (wpos[s.blk_u0[gn] + k] > need) need = wpos[s.blk_u0[gn] + k];
        s.bar_wait[gn] = (short)(n - need);
      }
      if (s.op_t[g] == OP_POLL) { ++n; pollpos = n; }
      if (s.op_t[g] == OP_PF) ++n;
      if (s.op_t[g] == OP_B) { ++n; bpos[s.op_a[g]] = n; }
      if (s.op_t[g] == OP_W) { if ((s.op_b[g] + 1) * 8 <= unit_np(s.op_a[g])) ++n; wpos[s.op_a[g]] = n; }
      if (s.chk[g]) s.chk_wait[g] = (short)(n - pollpos);
      int bk = (g >= NS1 ? 18 : 0) + s.bi[g];
      if (!bseen[bk]) { bseen[bk] = true; s.bwait[g] = (short)(n - bpos[bk]); }
      if (store_at[g]) { n += store_at[g]; storepos = n; }
      if (s.pub[g]) { s.pub_wait[g] = (short)(n - storepos); ++n; }
    }
  }
  for (int g = 0; g < NS; ++g) if (s.bar_wait[g] > 63 || s.bwait[g] > 63 || s.pub_wait[g] > 63 || s.chk_wait[g] > 63) s.ok = false;
  return s;
}
constexpr Sched SC = make_sched();
static_assert(SC.ok, "schedule");
}

namespace p1 {
constexpr int XS = 0;
constexpr int MODS = 92160;
constexpr int QP = 456;
constexpr int OS = 40960;
constexpr int OP = 160;
}
namespace p2 {
constexpr int OSM = 0;
constexpr int RED = 36864;
constexpr int RST = RED + 32 * 37 * 8;
constexpr int X1S = mlp::RING_BYTES;
constexpr int MV = X1S + 9216;
}
constexpr int SCRATCH = p2::MV + 4 * 144 * 4;
constexpr int LDS_BYTES = SCRATCH + 1024;

DI void mem_fence() { asm volatile("" ::: "memory"); }

template <int NN, int M0>
DI void p1_gemm(char* smem, bf16x8 (&wf)[2][18], rsrc_t wr, unsigned wso, rsrc_t wor, unsigned wo0, unsigned wo8, f32x4 (&acc)[2][5], int lane) {
  const int g = lane >> 4, m16 = lane & 15;
  const char* xs0 = smem + p1::XS + (g * 16 + (m16 ^ (2 * g))) * 16;
  const char* xs1 = smem + p1::XS + (g * 16 + (m16 ^ (2 * g + 1))) * 16;
  static_for<18>([&](auto kc) LAMBDA_INLINE {
    constexpr int k = decltype(kc)::value;
    if constexpr (k + 6 < 18) {
#pragma unroll
      for (int n = 0; n < NN; ++n) wf[n][k + 6] = ldfrag(wr, lane * 16, wso + (n * 18 + k + 6) * 1024);
    }
    if constexpr (NN == 1) wf[1][k] = ldfrag(wor, lane * 16, wo8 + k * 1024);
    if constexpr (k >= 12) {
      wf[0][2 * (k - 12)] = ldfrag(wor, lane * 16, wo0 + 2 * (k - 12) * 1024);
      wf[0][2 * (k - 12) + 1] = ldfrag(wor, lane * 16, wo0 + (2 * (k - 12) + 1) * 1024);
    }
    bf16x8 bf[5];
#pragma unroll
    for (int m = M0; m < 5; ++m) bf[m] = *(const bf16x8*)(((k & 1) ? xs1 : xs0) + (k * 5 + m) * 1024);
#pragma unroll
    for (int n = 0; n < NN; ++n)
#pragma unroll
      for (int m = M0; m < 5; ++m) acc[n][m] = __builtin_amdgcn_mfma_f32_16x16x32_bf16(wf[n][k], bf[m], acc[n][m], 0, 0, 0);
    mem_fence();
  });
}

extern "C" __global__ __launch_bounds__(512, 2) void k_main(const u16* __restrict__ x, const u16* __restrict__ w1, const u16* __restrict__ w3,
                                                 const u16* __restrict__ w2, char* __restrict__ ws, u16* __restrict__ out) {
  using namespace mlp;
  extern __shared__ __attribute__((aligned(16))) char smem[];
  const int tid = threadIdx.x, lane = tid & 63;
  const int wave = __builtin_amdgcn_readfirstlane(tid >> 6);
  const int b = blockIdx.x, xcd = b & 7, idx = b >> 3;
  const int mt = idx >> 2, ftl = idx & 3;
  const unsigned lds0 = (unsigned)(uintptr_t)(LAS char*)smem;
  unsigned* flags = (unsigned*)(ws + WS_FLAGS);
  STAMP_AT(0);
  const int a0 = ftl == 0 ? 0 : ftl == 1 ? 3 : ftl == 2 ? 2 : 5;
  const int c1 = ftl == 0 ? 4 : ftl == 1 ? 0 : ftl == 2 ? 6 : 0;
  const int e0 = ftl == 0 ? 7 : ftl == 1 ? 7 : ftl == 2 ? 0 : 3;
  const int fbase = (xcd * 4 + ftl) * 144;

  const char* sp[3];
  unsigned so[3];
  auto stage_setup = [&](auto cc_) LAMBDA_INLINE {
    constexpr int c = decltype(cc_)::value;
    const int lane = lane_now();
#pragma unroll
    for (int j = 0; j < (c == 2 ? 2 : 3); ++j) {
      int i = wave + 8 * j;
      if (c == 2) { if (i > 11) i = 11; } else { if (i > 17) i = 17; }
      const int ci = i * 64 + lane;
      if constexpr (c == 0) {
        int row = ci / 18, cc = ci % 18; if (cc > 15) cc = 15;
        sp[j] = (const char*)((cc >= 8 ? w3 : w1) + (size_t)row * F_ + fbase + a0 * 16 + (cc & 7) * 8);
      } else if constexpr (c == 1) {
        int row = ci / 12, cc = ci % 12;
        sp[j] = (const char*)((cc >= 6 ? w3 : w1) + (size_t)row * F_ + fbase + c1 * 16 + (cc >= 6 ? cc - 6 : cc) * 8);
      } else if constexpr (c == 2) {
        int row = ci >> 3, cc = ci & 7, pos = (cc >> 1) ^ ((row >> 1) & 1);
        sp[j] = (const char*)((pos >= 2 ? w3 : w1) + (size_t)row * F_ + fbase + e0 * 16 + (pos & 1) * 16 + (cc & 1) * 8);
      } else {
        int ip = i >= 9 ? i - 9 : i, c2 = ip * 64 + lane, row = c2 / 18, cc = c2 % 18;
        so[j] = (unsigned)(row * D_ + cc * 8) * 2u;
      }
    }
  };
  const u16* w2b = w2 + (size_t)xcd * 576 * D_ + ftl * 144;
  auto stage_round = [&](auto Uc, auto jc) LAMBDA_INLINE {
    constexpr int U = decltype(Uc)::value, j = decltype(jc)::value, c = unit_chunk(U);
    if constexpr (j == 0 && unit_k0(U) == 0) stage_setup(IC(c));
    const unsigned dst = lds0 + slot_of(U) * SLOT + (wave + 8 * j) * 1024;
    constexpr bool all = (j + 1) * 8 <= unit_np(U);
    if (all || wave + 8 * j < unit_np(U)) {
      if constexpr (c < 3) {
        glds16v(sp[j], dst);
      } else {
        constexpr int pA = 2 * unit_k0(U), pB = pA + 2;
        const bool h1 = wave + 8 * j >= 9;
        const int r0 = h1 ? pos_row(pB) : pos_row(pA);
        constexpr int dA = pos_row(pA + 1) - pos_row(pA) - 16, dB = pos_row(pB + 1) - pos_row(pB) - 16;
        unsigned vo = so[j];
        if constexpr (dA != 0 || dB != 0) vo += (so[j] >= 16u * D_ * 2u) ? (unsigned)((h1 ? dB : dA) * D_ * 2) : 0u;
        glds16(vo, w2b + (size_t)r0 * D_, dst);
      }
    }
    if constexpr (c < 3) sp[j] += (size_t)unit_nk(U) * 32 * F_ * 2;
  };

  const int g = lane >> 4, m16 = lane & 15;
  bf16x8 wf[2][18];
  {
    using namespace p1;
    const int rt = xcd * 4 + (idx >> 3), hg = idx & 7;
    const int r0 = rt * 64 - 16;
    const rsrc_t xr = mk_rsrc(ws + WS_N1, S_ * D_ * 2), mr = mk_rsrc(ws + WS_MODF, 65536 + 576 * 4);
    const rsrc_t wr = mk_rsrc(ws + WS_WQF, 8 * 14 * 18 * 1024);
    const int r8 = lane >> 3, ch = lane & 7;
    u32x4 sa[2][2];
#pragma unroll
    for (int part = 0; part < 2; ++part) {
      const unsigned so_ = 65536 + (part ? 8 : wave) * 256;
      sa[part][0] = ld16(mr, ch * 32, so_); sa[part][1] = ld16(mr, ch * 32 + 16, so_);
    }
    u32x4 xv[12];
#pragma unroll
    for (int i = 0; i < 12; ++i) {
      const int rg = i < 10 ? i : i == 10 ? wave : 8 + (wave & 1), ln = i < 10 ? wave : 8;
      if (i < 11 || wave >= 6) xv[i] = ld16(xr, r8 * 1152 + ch * 16, (unsigned)((r0 + rg * 8) * 1152 + ln * 128));
    }
    f32x4 mvv;
    if (wave < 3) mvv = ldf4(mr, tid < 144 ? ((2 + tid / 36) * D_ + (tid % 36) * 4) * 4 : 0x40000000, ftl * 576);
    barrier_raw();
    STAMP_AT(17);
    const int nf0 = wave < 4 ? 4 + 2 * wave : wave < 6 ? 2 * (wave - 4) : 6 + wave;
    const unsigned wso = (unsigned)((hg * 14 + nf0) * 18 * 1024);
    auto w_load = [&](int k) LAMBDA_INLINE {
      wf[0][k] = ldfrag(wr, lane * 16, wso + k * 1024);
      if (wave < 6) wf[1][k] = ldfrag(wr, lane * 16, wso + (18 + k) * 1024);
      mem_fence();
    };
    w_load(0); w_load(1);
    STAMP_AT(18);
    {
      const int gq = ch & 3, kb = ch >> 2, swz = 2 * gq + kb;
#pragma unroll
      for (int i = 0; i < 12; ++i) {
        const int part = i >= 10, ln = part ? 8 : wave;
        const int rg = i < 10 ? i : i == 10 ? wave : 8 + (wave & 1);
        if (i < 11 || wave >= 6) {
          u32x4 o;
#pragma unroll
          for (int j = 0; j < 4; ++j) {
            const unsigned m0 = sa[part][j >> 1][(2 * j) & 3], m1 = sa[part][j >> 1][(2 * j + 1) & 3];
            o[j] = pack_bf16(bflo(xv[i][j]) * bfhi(m0) + bflo(m0), bfhi(xv[i][j]) * bfhi(m1) + bflo(m1));
          }
          *(u32x4*)(smem + XS + ((2 * ln + kb) * 5 + (rg >> 1)) * 1024 + gq * 256 + (((rg & 1) * 8 + r8) ^ swz) * 16) = o;
        }
        if (i < 8 && (i & 1)) w_load(2 + (i >> 1));
      }
    }
    if (tid < 144) *(f32x4*)(smem + p2::MV + tid * 16) = mvv;
    STAMP_AT(19);
    lds_barrier();
    STAMP_AT(1);
    const rsrc_t wor = mk_rsrc(ws + WS_WOF, 36 * 18 * 1024);
    const unsigned wo0 = (unsigned)((ftl * 9 + wave) * 18 * 1024), wo8 = (unsigned)((ftl * 9 + 8) * 18 * 1024);
    f32x4 acc[2][5] = {};
    if (wave < 4) p1_gemm<2, 0>(smem, wf, wr, wso, wor, wo0, wo8, acc, lane);
    else if (wave < 6) p1_gemm<2, 1>(smem, wf, wr, wso, wor, wo0, wo8, acc, lane);
    else {
      p1_gemm<1, 0>(smem, wf, wr, wso, wor, wo0, wo8, acc, lane);
    }
    STAMP_AT(20);
    lds_barrier();
    STAMP_AT(2);
#pragma unroll
    for (int n = 0; n < 2; ++n)
#pragma unroll
      for (int m = 0; m < 5; ++m) {
        if (n == 1 && wave >= 6) continue;
        u32x2 v = {pack_bf16(acc[n][m][0], acc[n][m][1]), pack_bf16(acc[n][m][2], acc[n][m][3])};
        *(u32x2*)(smem + (m * 16 + m16) * QP + ((nf0 + n) * 16 + 4 * g) * 2) = v;
      }
    lds_barrier();
    STAMP_AT(21);
    auto wo_load = [&](int k) LAMBDA_INLINE { wf[0][k] = ldfrag(wor, lane * 16, wo0 + k * 1024); mem_fence(); };
    {
      typedef __bf16 bf2 __attribute__((ext_vector_type(2)));
      const int rl = (wave >> 1) * 16 + m16, hd = (wave & 1) * 2 + (g & 1), hw = lane >> 5;
      const int grow = rt * 64 + rl;
      const unsigned* qrow = (const unsigned*)(smem + (rl + 16) * QP) + hd * 9;
      unsigned qv[9];
#pragma unroll
      for (int d = 0; d < 9; ++d) qv[d] = qrow[d];
      wo_load(12); wo_load(13);
      float lg[4];
#pragma unroll
      for (int w = 0; w < 4; ++w) {
        const int ww = hw * 4 + w;
        const unsigned* kr = (const unsigned*)(smem + (rl + 16 - 2 * ww) * QP) + 36 + hd * 9;
        float a = 0.f;
#pragma unroll
        for (int d = 0; d < 9; ++d) a = __builtin_amdgcn_fdot2_f32_bf16(__builtin_bit_cast(bf2, qv[d]), __builtin_bit_cast(bf2, kr[d]), a, false);
        a *= 0.23570226039551584f;
        lg[w] = (grow - 2 * ww >= 0) ? a : -1e9f;
        wo_load(14 + w);
      }
      STAMP_AT(22);
      auto both = [&](float v) LAMBDA_INLINE { return __builtin_amdgcn_permlane32_swap(__float_as_uint(v), __float_as_uint(v), false, false); };
      float l8[8];
#pragma unroll
      for (int w = 0; w < 4; ++w) { const auto r = both(lg[w]); l8[w] = __uint_as_float(r[0]); l8[4 + w] = __uint_as_float(r[1]); }
      const float mxv = fmaxf(fmaxf(fmaxf(l8[0], l8[1]), fmaxf(l8[2], l8[3])), fmaxf(fmaxf(l8[4], l8[5]), fmaxf(l8[6], l8[7])));
      float sum = 0.f;
#pragma unroll
      for (int w = 0; w < 8; ++w) { l8[w] = __builtin_amdgcn_exp2f((l8[w] - mxv) * 1.44269504089f); sum += l8[w]; }
      const float inv = __builtin_amdgcn_rcpf(sum);
      float o[10] = {};
#pragma unroll
      for (int w = 0; w < 8; w += 2) {
        const bf2 pp = __builtin_bit_cast(bf2, pack_bf16(l8[w] * inv, l8[w + 1] * inv));
        const unsigned* v0 = (const unsigned*)(smem + (rl + 16 - 2 * w) * QP) + 72 + hd * 9 + hw * 5;
        const unsigned* v1 = (const unsigned*)(smem + (rl + 14 - 2 * w) * QP) + 72 + hd * 9 + hw * 5;
#pragma unroll
        for (int d = 0; d < 5; ++d) {
          const unsigned a = v0[d], c = v1[d];
          o[2 * d] = __builtin_amdgcn_fdot2_f32_bf16(pp, __builtin_bit_cast(bf2, __builtin_amdgcn_perm(c, a, 0x05040100u)), o[2 * d], false);
          o[2 * d + 1] = __builtin_amdgcn_fdot2_f32_bf16(pp, __builtin_bit_cast(bf2, __builtin_amdgcn_perm(c, a, 0x07060302u)), o[2 * d + 1], false);
        }
      }
      STAMP_AT(23);
      unsigned* orow = (unsigned*)(smem + OS + rl * OP) + hd * 9 + hw * 5;
#pragma unroll
      for (int d = 0; d < 5; ++d) if (d < 4 || hw == 0) orow[d] = pack_bf16(o[2 * d], o[2 * d + 1]);
    }
    lds_barrier();
    STAMP_AT(3);
    const rsrc_t ors = mk_rsrc(ws + WS_OF, S_ * D_ * 2);
#pragma unroll
    for (int it = 0; it < 2; ++it) {
      const int pc = tid + it * 512;
      if (pc < 576) {
        const int row = pc & 63, c = pc >> 6;
        const u32x4 v = *(const u32x4*)(smem + OS + row * OP + c * 16);
        const int c8 = hg * 9 + c, mf = rt * 4 + (row >> 4);
        st16_sc1(ors, (unsigned)(((mf * 18 + (c8 >> 2)) * 64 + (c8 & 3) * 16 + (row & 15)) * 16), 0, v);
      }
    }
    wait_vm<0>();
    barrier_raw();
    if (tid == 0) set_flag(flags + FL_O + rt * 32 + hg, 1u);
    STAMP_AT(4);
  }

  {
    using namespace p2;
    const int rt = mt * 8 + xcd, nt = ftl;
    u32x2 xr[3];
#pragma unroll
    for (int t = 0; t < 3; ++t) {
      const int nf = t < 2 ? wave : 8, mi = t < 2 ? t : (wave & 1);
      xr[t] = *(const u32x2*)(x + (size_t)(rt * 32 + mi * 16 + m16) * D_ + nt * 144 + nf * 16 + 4 * g);
    }
    wait_flags(flags + FL_O + (rt >> 1) * 32, 8, 1u, lane);
    STAMP_AT(5);
    const rsrc_t ors = mk_rsrc(ws + WS_OF, S_ * D_ * 2);
    u32x4 ov[5];
#pragma unroll
    for (int i = 0; i < 5; ++i) {
      const int f = wave + 8 * i;
      if (f < 36) ov[i] = ld16_sc1(ors, lane * 16, (unsigned)((rt * 36 + f) * 1024));
    }
#pragma unroll
    for (int i = 0; i < 5; ++i) {
      const int f = wave + 8 * i;
      if (f < 36) *(u32x4*)(smem + OSM + f * 1024 + lane * 16) = ov[i];
    }
    lds_barrier();
    STAMP_AT(6);
    f32x4 acc[3] = {};
    bf16x8 ob[2][18];
#pragma unroll
    for (int k = 0; k < 3; ++k) { ob[0][k] = *(const bf16x8*)(smem + OSM + k * 1024 + lane * 16); ob[1][k] = *(const bf16x8*)(smem + OSM + (18 + k) * 1024 + lane * 16); }
    static_for<18>([&](auto kc) LAMBDA_INLINE {
      constexpr int k = decltype(kc)::value;
      if constexpr (k + 3 < 18) {
        ob[0][k + 3] = *(const bf16x8*)(smem + OSM + (k + 3) * 1024 + lane * 16);
        ob[1][k + 3] = *(const bf16x8*)(smem + OSM + (21 + k) * 1024 + lane * 16);
      }
      acc[0] = __builtin_amdgcn_mfma_f32_16x16x32_bf16(wf[0][k], ob[0][k], acc[0], 0, 0, 0);
      acc[1] = __builtin_amdgcn_mfma_f32_16x16x32_bf16(wf[0][k], ob[1][k], acc[1], 0, 0, 0);
      if (wave >= 6) acc[2] = __builtin_amdgcn_mfma_f32_16x16x32_bf16(wf[1][k], (wave & 1) ? ob[1][k] : ob[0][k], acc[2], 0, 0, 0);
      if constexpr (k < 3 * NPRE) stage_round(IC(k / 3), IC(k % 3));
    });
    float2* red = (float2*)(smem + RED);
    const float* mv = (const float*)(smem + MV);
    f32x4 xv[3];
#pragma unroll
    for (int t = 0; t < 3; ++t) {
      if (t == 2 && wave < 6) continue;
      const int nf = t < 2 ? wave : 8, mi = t < 2 ? t : (wave & 1);
      const f32x4 gv = *(const f32x4*)(mv + nf * 16 + 4 * g);
      u32x2 xb = {pack_bf16(bflo(xr[t][0]) + gv[0] * acc[t][0], bfhi(xr[t][0]) + gv[1] * acc[t][1]),
                  pack_bf16(bflo(xr[t][1]) + gv[2] * acc[t][2], bfhi(xr[t][1]) + gv[3] * acc[t][3])};
      *(u32x2*)(smem + X1S + ((nf * 2 + mi) * 64 + lane) * 8) = xb;
      f32x4 v = {bflo(xb[0]), bfhi(xb[0]), bflo(xb[1]), bfhi(xb[1])};
      xv[t] = v;
      const float mu = (v[0] + v[1] + v[2] + v[3]) * 0.25f;
      const float q = (v[0] - mu) * (v[0] - mu) + (v[1] - mu) * (v[1] - mu) + (v[2] - mu) * (v[2] - mu) + (v[3] - mu) * (v[3] - mu);
      red[(mi * 16 + m16) * 37 + nf * 4 + g] = make_float2(mu, q);
    }
    lds_barrier();
    STAMP_AT(7);
    GAS u64* stg = (GAS u64*)(flags + FL_ST) + (size_t)rt * 128;
    if (wave == 0) {
      const int row = lane & 31, hf = lane >> 5;
      float2 e[18];
#pragma unroll
      for (int i = 0; i < 18; ++i) e[i] = red[row * 37 + hf * 18 + i];
      float mu = 0.f;
#pragma unroll
      for (int i = 0; i < 18; ++i) mu += e[i].x;
      mu += __shfl_xor(mu, 32);
      mu *= (1.f / 36.f);
      float q = 0.f;
#pragma unroll
      for (int i = 0; i < 18; ++i) q += e[i].y + 4.f * (e[i].x - mu) * (e[i].x - mu);
      q += __shfl_xor(q, 32);
      if (lane < 32) __hip_atomic_store(stg + nt * 32 + row, ((u64)(__float_as_uint(q) | 0x80000000u) << 32) | __float_as_uint(mu), RLX_AGENT);
      u64 e0v = 0, e1v = 0;
#pragma nounroll
      for (unsigned spin = 0; spin < SPIN_CAP; ++spin) {
        e0v = __hip_atomic_load(stg + lane, RLX_AGENT);
        e1v = __hip_atomic_load(stg + 64 + lane, RLX_AGENT);
        if (__all((e0v >> 63) && (e1v >> 63))) break;
        __builtin_amdgcn_s_sleep(1);
      }
      const float m0 = __uint_as_float((unsigned)e0v), m1 = __uint_as_float((unsigned)e1v);
      const float q0 = __uint_as_float((unsigned)(e0v >> 32) & 0x7fffffffu), q1 = __uint_as_float((unsigned)(e1v >> 32) & 0x7fffffffu);
      float ms = m0 + m1; ms += __shfl_xor(ms, 32);
      const float mean = 0.25f * ms;
      float qs = q0 + q1 + 144.f * ((m0 - mean) * (m0 - mean) + (m1 - mean) * (m1 - mean));
      qs += __shfl_xor(qs, 32);
      const float rstd = __builtin_amdgcn_rsqf(qs * (1.f / D_) + 1e-5f);
      if (lane < 32) ((float2*)(smem + RST))[row] = make_float2(rstd, -mean * rstd);
    }
    lds_barrier();
    STAMP_AT(8);
    const rsrc_t hrs2 = mk_rsrc(ws + WS_H2F, S_ * D_ * 2);
#pragma unroll
    for (int t = 0; t < 3; ++t) {
      if (t == 2 && wave < 6) continue;
      const int nf = t < 2 ? wave : 8, mi = t < 2 ? t : (wave & 1);
      const float2 st = ((const float2*)(smem + RST))[mi * 16 + m16];
      const f32x4 sbv = *(const f32x4*)(mv + 144 + nf * 16 + 4 * g), cbv = *(const f32x4*)(mv + 288 + nf * 16 + 4 * g);
      float h[4];
#pragma unroll
      for (int j = 0; j < 4; ++j) h[j] = (xv[t][j] * st.x + st.y) * cbv[j] + sbv[j];
      const int nfg = nt * 9 + nf;
      u32x2 o = {pack_bf16(h[0], h[1]), pack_bf16(h[2], h[3])};
      st8_sc1(hrs2, (unsigned)((g * 32 + mi * 16 + m16) * 16), (unsigned)((rt * 18 + (nfg >> 1)) * 2048 + (nfg & 1) * 8), o);
    }
    wait_vm<0>();
    barrier_raw();
    if (tid == 0) set_flag(flags + FL_H2 + rt * 32 + nt, 1u);
    STAMP_AT(9);
  }

  bf16x8 xf[18][2];
  const int mf0 = (mt * 8 + wave) * 2;
  const char* xb = ws + WS_H2F + (size_t)(mt * 8 + wave) * 36 * 1024;
  const char* hb = ws + WS_HF + ((size_t)mf0 * 8 + xcd) * 18432;
  const unsigned* hfl = flags + FL_H + ((xcd * 8 + mt) * 8 + wave) * 16;
  const unsigned l16 = lane * 16;
  unsigned flagv;
  auto bload = [&](auto ac, auto mc) LAMBDA_INLINE {
    constexpr int a = decltype(ac)::value, mi = decltype(mc)::value;
    if constexpr (a < 18) {
      constexpr int o = (a * 2 + mi) * 1024;
      gload16_sc1<o % 4096>(xf[a][mi], l16, xb + (o / 4096) * 4096);
    } else {
      constexpr int o = (a - 18) * 1024;
      gload16_sc1<o % 4096>(xf[a - 18][mi], l16, hb + mi * 8 * 18432 + (o / 4096) * 4096);
    }
  };
  wait_flags(flags + FL_H2 + (mt * 8 + wave) * 32, 4, 1u, lane);
  STAMP_AT(10);
  static_for<SC.pro_n>([&](auto ic) LAMBDA_INLINE { constexpr int i = decltype(ic)::value; bload(IC(SC.pro_a[i]), IC(SC.pro_b[i])); });

  unsigned ubase[3][2];
  auto block_barrier = [&](auto Gc) LAMBDA_INLINE {
    constexpr int G = decltype(Gc)::value, u0 = SC.blk_u0[G], nu = SC.blk_nu[G], c = unit_chunk(u0);
    sched_fence();
    wait_vm<SC.bar_wait[G]>();
    barrier_raw();
    const int ln = lane_now(), p = ln & 3, q = (ln >> 2) & 3, blk = (ln >> 4) & 1, hh = ln >> 5;
#pragma unroll
    for (int k = 0; k < nu; ++k) {
      const unsigned sb = lds0 + slot_of(u0 + k) * SLOT + 8 * p;
      if constexpr (c == 0) ubase[k][0] = sb + (4 * hh + q) * 288 + blk * 128;
      else if constexpr (c == 1) ubase[k][0] = sb + (4 * hh + q) * 192 + blk * 96;
      else if constexpr (c == 2) {
        ubase[k][0] = sb + (4 * hh + q) * 128 + blk * 64 + 32 * (q >> 1);
        ubase[k][1] = sb + (4 * hh + q) * 128 + blk * 64 + 32 * (1 - (q >> 1));
      } else ubase[k][0] = sb + (16 * hh + 4 * blk + q) * 288;
      asm volatile("" : "+v"(ubase[k][0]));
      if constexpr (c == 2) asm volatile("" : "+v"(ubase[k][1]));
    }
    sched_fence();
  };
  auto afrag = [&](auto Gc) LAMBDA_INLINE {
    constexpr int G = decltype(Gc)::value, U = SC.unit[G], c = unit_chunk(U), k = SC.ub[G], kl = SC.kl[G], sb = SC.sub[G];
    if constexpr (c == 0) {
      constexpr unsigned o = (kl * 32 + 8 * sb) * 288 + SC.tile[G] * 32;
      return tr_frag(ubase[k][0] + o, ubase[k][0] + o + 16 * 288);
    } else if constexpr (c == 1) {
      constexpr unsigned o = (kl * 32 + 8 * sb) * 192 + (SC.tile[G] - 4) * 32;
      return tr_frag(ubase[k][0] + o, ubase[k][0] + o + 16 * 192);
    } else if constexpr (c == 2) {
      constexpr unsigned o = (kl * 32 + 8 * sb) * 128;
      return tr_frag(ubase[k][SC.tile[G] - 7] + o, ubase[k][SC.tile[G] - 7] + o + 16 * 128);
    } else {
      constexpr unsigned o = kl * 32 * 288 + SC.tile[G] * 32;
      return tr_frag(ubase[k][0] + o, ubase[k][0] + o + 8 * 288);
    }
  };
  f32x16 acc[4];
  f32x4 acc2[9][2];
  bf16x8 af[PF + 1];
  float ta[2], tp[2], hv[2];
  u32x4 pk;
  const rsrc_t hrs = mk_rsrc(ws + WS_HF, 128 * 8 * 18432);
  const rsrc_t prs = mk_rsrc(ws + WS_PART, 64 * 32 * 9 * 1024);
  const unsigned hso = (unsigned)((mf0 * 8 + xcd) * 18432);
  const unsigned tile = (unsigned)(mt * 4 + ftl);
  auto epi = [&](auto Gc) LAMBDA_INLINE {
    constexpr int G = decltype(Gc)::value;
    if constexpr (SC.eC[G] >= 0) {
      constexpr int e = SC.eC[G], i = e >> 3, j = e & 7;
      hv[j & 1] = tp[e & 1] * ta[e & 1];
      if constexpr (j & 1) {
        pk[j >> 1] = pack_bf16(hv[0], hv[1]);
        asm volatile("" : "+v"(pk));
      } else {
        asm volatile("" : "+v"(hv[0]));
      }
      if constexpr (j == 7) {
        const int ln = lane_now();
        const int pos = i < 4 ? 4 * ftl + i : i < 7 ? 16 + 3 * ftl + (i - 4) : 28 + 2 * ftl + (i - 7);
        st16_sc1(hrs, (unsigned)(((ln >> 4) & 1) * 8 * 18432 + (ln >> 5) * 256 + (ln & 15) * 16), hso + pos * 512, pk);
      }
    }
    if constexpr (SC.eB[G] >= 0) {
      constexpr int e = SC.eB[G];
      ta[e & 1] = __builtin_amdgcn_rcpf(1.f + ta[e & 1]);
      asm volatile("" : "+v"(ta[e & 1]));
    }
    if constexpr (SC.eA[G] >= 0) {
      constexpr int e = SC.eA[G], i = e >> 3, j = e & 7, st = tile_set(i);
      const float gv = acc[st][j];
      tp[e & 1] = gv * acc[st][8 + j];
      ta[e & 1] = __builtin_amdgcn_exp2f(gv * -1.44269504089f);
      asm volatile("" : "+v"(ta[e & 1]), "+v"(tp[e & 1]));
    }
  };
  auto part_store = [&](auto nc) LAMBDA_INLINE {
    constexpr int nf = decltype(nc)::value;
    u32x4 v = {pack_bf16(acc2[nf][0][0], acc2[nf][0][1]), pack_bf16(acc2[nf][0][2], acc2[nf][0][3]),
               pack_bf16(acc2[nf][1][0], acc2[nf][1][1]), pack_bf16(acc2[nf][1][2], acc2[nf][1][3])};
    if (wave == xcd) *(u32x4*)(smem + OWN + nf * 1024 + l16) = v;
    else st16_sc1(prs, l16, (((wave * 8 + xcd) * 32 + tile) * 9 + nf) * 1024, v);
  };

  block_barrier(IC(0));
  static_for<PF>([&](auto ic) LAMBDA_INLINE { constexpr int G = decltype(ic)::value; af[G % (PF + 1)] = afrag(IC(G)); });
  static_for<NS>([&](auto ic) LAMBDA_INLINE {
    constexpr int G = decltype(ic)::value, GN = G + PF, kk = SC.bi[G];
    if constexpr (G == NS1) STAMP_AT(11);
    if constexpr (G == 1) STAMP_AT(16);
    if constexpr (GN < NS) {
      if constexpr (SC.blk_u0[GN] >= 0) block_barrier(IC(GN));
    }
    if constexpr (SC.op_t[G] == OP_W) stage_round(IC(SC.op_a[G]), IC(SC.op_b[G]));
    if constexpr (SC.op_t[G] == OP_B) bload(IC(SC.op_a[G]), IC(SC.op_b[G]));
    if constexpr (SC.op_t[G] == OP_POLL) gload4_sc1(flagv, (lane_now() & 3) * 4, hfl);
    if constexpr (SC.op_t[G] == OP_PF) {
      int pc = SC.op_a[G] * 8 + wave; if (pc > 19) pc = 19;
      glds16(l16, (const char*)w2 + (size_t)(xcd * 576 + idx * 18) * 1152 + pc * 1024 + (pc == 19 && wave > 3 ? 256 : 0), lds0 + SCRATCH);
    }
    if constexpr (SC.chk[G] != 0) {
      asm volatile("s_waitcnt vmcnt(%1)" : "+v"(flagv) : "i"(SC.chk_wait[G]) : "memory");
      if (!__all(flagv >= (unsigned)SC.chk[G])) wait_flags(hfl, 4, (unsigned)SC.chk[G], lane_now());
    }
    if constexpr (SC.bwait[G] >= 0)
      asm volatile("s_waitcnt vmcnt(%2)" : "+v"(xf[kk][0]), "+v"(xf[kk][1]) : "i"(SC.bwait[G]) : "memory");
    if constexpr (G < NS1) {
      constexpr int st = tile_set(SC.tile[G]);
      const f32x16 z = {};
      acc[st] = __builtin_amdgcn_mfma_f32_32x32x16_bf16(af[G % (PF + 1)], xf[kk][SC.sub[G]], SC.first[G] ? z : acc[st], 0, 0, 0);
      af[GN % (PF + 1)] = afrag(IC(GN));
    } else {
      constexpr int nf = SC.tile[G];
      const f32x4 z = {};
      acc2[nf][0] = __builtin_amdgcn_mfma_f32_16x16x32_bf16(af[G % (PF + 1)], xf[kk][0], SC.first[G] ? z : acc2[nf][0], 0, 0, 0);
      if constexpr (GN < NS) af[GN % (PF + 1)] = afrag(IC(GN));
      acc2[nf][1] = __builtin_amdgcn_mfma_f32_16x16x32_bf16(af[G % (PF + 1)], xf[kk][1], SC.first[G] ? z : acc2[nf][1], 0, 0, 0);
    }
    epi(IC(G));
    if constexpr (SC.pst[G] >= 0) part_store(IC(SC.pst[G]));
    if constexpr (SC.pub[G] != 0) {
      wait_vm<SC.pub_wait[G]>();
      if (lane_now() == 0) set_flag((unsigned*)hfl + ftl, (unsigned)SC.pub[G]);
    }
    sched_fence();
  });
  STAMP_AT(12);

  {
    part_store(IC(8));
    const int ln = lane_now(), gg = ln >> 4, mm = ln & 15;
    wait_vm<0>();
    if (ln == 0 && wave != xcd) set_flag(flags + FL_P + (tile * 8 + wave) * 16 + xcd, 1u);
    STAMP_AT(13);
    lds_barrier();
    u32x4 pv[8]; u32x2 pe[8];
    const GAS unsigned* fp = (const GAS unsigned*)(flags + FL_P + (tile * 8 + xcd) * 16) + (ln & 7);
    unsigned pend = 0xffu & ~(1u << xcd);
#pragma unroll
    for (int s = 0; s < 8; ++s) {
      pv[s] = *(const u32x4*)(smem + OWN + wave * 1024 + ln * 16);
      pe[s] = *(const u32x2*)(smem + OWN + 8 * 1024 + ln * 16 + (wave & 1) * 8);
    }
#pragma nounroll
    for (unsigned spin = 0; spin < SPIN_CAP && pend; ++spin) {
      const unsigned ready = (unsigned)__builtin_amdgcn_readfirstlane((unsigned)__ballot(__hip_atomic_load(fp, RLX_AGENT) != 0u)) & pend;
      mem_fence();
#pragma unroll
      for (int s = 0; s < 8; ++s) {
        if ((ready >> s) & 1u) {
          pv[s] = ld16_sc1(prs, ln * 16, (((xcd * 8 + s) * 32 + tile) * 9 + wave) * 1024);
          if (wave >= 6) pe[s] = ld8_sc1(prs, ln * 16 + (wave & 1) * 8, (((xcd * 8 + s) * 32 + tile) * 9 + 8) * 1024);
        }
      }
      pend &= ~ready;
      if (!ready) __builtin_amdgcn_s_sleep(1);
    }
    STAMP_AT(14);
    float y[3][4] = {};
#pragma unroll
    for (int s = 0; s < 8; ++s) {
#pragma unroll
      for (int t = 0; t < 2; ++t) {
        y[t][0] += bflo(pv[s][2 * t]); y[t][1] += bfhi(pv[s][2 * t]); y[t][2] += bflo(pv[s][2 * t + 1]); y[t][3] += bfhi(pv[s][2 * t + 1]);
      }
      if (wave >= 6) { y[2][0] += bflo(pe[s][0]); y[2][1] += bfhi(pe[s][0]); y[2][2] += bflo(pe[s][1]); y[2][3] += bfhi(pe[s][1]); }
    }
#pragma unroll
    for (int t = 0; t < 3; ++t) {
      if (t == 2 && wave < 6) continue;
      const int nf = t < 2 ? wave : 8, mi = t < 2 ? t : (wave & 1);
      const u32x2 x1 = *(const u32x2*)(smem + p2::X1S + ((nf * 2 + mi) * 64 + ln) * 8);
      const f32x4 gv = *(const f32x4*)(smem + p2::MV + (432 + nf * 16 + 4 * gg) * 4);
      u32x2 o = {pack_bf16(bflo(x1[0]) + gv[0] * y[t][0], bfhi(x1[0]) + gv[1] * y[t][1]),
                 pack_bf16(bflo(x1[1]) + gv[2] * y[t][2], bfhi(x1[1]) + gv[3] * y[t][3])};
      *(u32x2*)(out + (size_t)((mt * 8 + xcd) * 32 + mi * 16 + mm) * D_ + ftl * 144 + nf * 16 + 4 * gg) = o;
    }
  }
  STAMP_AT(15);
}

extern "C" __global__ void k_tail() {}

extern "C" void kernel_launch(void* const* d_in, const int* in_sizes, int n_in, void* d_out, int out_size, void* d_ws,
                              size_t ws_size, hipStream_t stream) {
  const u16* x = (const u16*)d_in[0];
  const u16* wqkv = (const u16*)d_in[1];
  const u16* wo = (const u16*)d_in[2];
  const u16* w1 = (const u16*)d_in[4];
  const u16* w3 = (const u16*)d_in[5];
  const u16* w2 = (const u16*)d_in[6];
  const u16* c = (const u16*)d_in[7];
  const u16* wmod = (const u16*)d_in[8];
  (void)hipFuncSetAttribute((const void*)k_main, hipFuncAttributeMaxDynamicSharedMemorySize, LDS_BYTES);
  k_pre<<<k0::NB, 512, 0, stream>>>(x, wqkv, wo, c, wmod, (char*)d_ws);
  k_main<<<256, 512, LDS_BYTES, stream>>>(x, w1, w3, w2, (char*)d_ws, (u16*)d_out);
}
```

```cpp
#include <hip/hip_runtime.h>
#include <hip/hip_bf16.h>
#include <stdint.h>
#include <utility>

typedef unsigned short u16;
typedef unsigned long long u64;
typedef short bf16x8 __attribute__((ext_vector_type(8)));
typedef short s16x4 __attribute__((ext_vector_type(4)));
typedef float f32x4 __attribute__((ext_vector_type(4)));
typedef float f32x16 __attribute__((ext_vector_type(16)));
typedef unsigned u32x4 __attribute__((ext_vector_type(4)));
typedef unsigned u32x2 __attribute__((ext_vector_type(2)));
#define LAS __attribute__((address_space(3)))
#define GAS __attribute__((address_space(1)))
#define DI __device__ __forceinline__
#define LAMBDA_INLINE __attribute__((always_inline))
#define IC(x) std::integral_constant<int, (x)>{}
#define RLX_AGENT __ATOMIC_RELAXED, __HIP_MEMORY_SCOPE_AGENT

constexpr int S_ = 2048, D_ = 576, F_ = 4608;

template <int... Is, class Fn> DI void static_for_impl(std::integer_sequence<int, Is...>, Fn&& f) { (f(std::integral_constant<int, Is>{}), ...); }
template <int N, class Fn> DI void static_for(Fn&& f) { static_for_impl(std::make_integer_sequence<int, N>{}, f); }

DI float bflo(unsigned v) { return __uint_as_float(v << 16); }
DI float bfhi(unsigned v) { return __uint_as_float(v & 0xffff0000u); }
DI unsigned pack_bf16(float lo, float hi) {
  typedef __bf16 bf2 __attribute__((ext_vector_type(2)));
  typedef float f2 __attribute__((ext_vector_type(2)));
  f2 v = {lo, hi};
  bf2 r = __builtin_convertvector(v, bf2);
  return __builtin_bit_cast(unsigned, r);
}
DI float rbf(float f) { return __uint_as_float(pack_bf16(f, 0.f) << 16); }
DI float inv_std(float var) { return __builtin_amdgcn_rcpf(rbf(__builtin_amdgcn_sqrtf(rbf(rbf(var) + 1e-5f)))); }
DI unsigned resid2(unsigned x, float g0, float g1, float y0, float y1) {
  const unsigned a = pack_bf16(y0, y1), b = pack_bf16(g0 * bflo(a), g1 * bfhi(a));
  return pack_bf16(bflo(x) + bflo(b), bfhi(x) + bfhi(b));
}
DI float dot2v(unsigned a, unsigned b, float c) {
  float r;
  asm volatile("v_dot2_f32_bf16 %0, %1, %2, %3" : "=v"(r) : "v"(a), "v"(b), "v"(c));
  return r;
}
DI unsigned cvt2v(float lo, float hi) {
  unsigned r;
  asm volatile("v_cvt_pk_bf16_f32 %0, %1, %2" : "=v"(r) : "v"(lo), "v"(hi));
  return r;
}
DI float silu_f(float g) { return g * __builtin_amdgcn_rcpf(1.f + __builtin_amdgcn_exp2f(g * -1.44269504089f)); }

template <int N> DI void wait_vm() { asm volatile("s_waitcnt vmcnt(%0)" ::"i"(N) : "memory"); }
DI void barrier_raw() { asm volatile("" ::: "memory"); __builtin_amdgcn_s_barrier(); asm volatile("" ::: "memory"); }
DI void lds_barrier() { asm volatile("s_waitcnt lgkmcnt(0)" ::: "memory"); __builtin_amdgcn_s_barrier(); asm volatile("" ::: "memory"); }
DI void sched_fence() { __builtin_amdgcn_sched_barrier(0); }
DI int lane_now() {
  int l = __builtin_amdgcn_mbcnt_hi(~0u, __builtin_amdgcn_mbcnt_lo(~0u, 0u));
  asm volatile("" : "+v"(l));
  return l;
}

DI void glds16(unsigned voff, const void* sbase, unsigned lds_dst) {
  unsigned keep;
  asm volatile("s_mov_b32 %0, m0\n\ts_mov_b32 m0, %3\n\ts_nop 0\n\tglobal_load_lds_dwordx4 %1, %2\n\ts_mov_b32 m0, %0"
               : "=&s"(keep) : "v"(voff), "s"(sbase), "s"(lds_dst) : "memory");
}
DI void glds16v(const void* vaddr, unsigned lds_dst) {
  unsigned keep;
  asm volatile("s_mov_b32 %0, m0\n\ts_mov_b32 m0, %2\n\ts_nop 0\n\tglobal_load_lds_dwordx4 %1, off\n\ts_mov_b32 m0, %0"
               : "=&s"(keep) : "v"(vaddr), "s"(lds_dst) : "memory");
}
template <int IMM> DI void gload16_sc1(bf16x8& dst, unsigned voff, const void* sbase) {
  asm volatile("global_load_dwordx4 %0, %1, %2 offset:%3 sc1" : "=v"(dst) : "v"(voff), "s"(sbase), "i"(IMM) : "memory");
}
DI void gload4_sc1(unsigned& dst, unsigned voff, const void* sbase) {
  asm volatile("global_load_dword %0, %1, %2 sc1" : "=v"(dst) : "v"(voff), "s"(sbase) : "memory");
}
DI s16x4 tr_read(unsigned addr) { return __builtin_amdgcn_ds_read_tr16_b64_v4i16((LAS s16x4*)(uintptr_t)addr); }
DI bf16x8 tr_frag(unsigned a0, unsigned a1) {
  s16x4 lo = tr_read(a0), hi = tr_read(a1);
  return __builtin_shufflevector(lo, hi, 0, 1, 2, 3, 4, 5, 6, 7);
}
typedef __amdgpu_buffer_rsrc_t rsrc_t;
DI rsrc_t mk_rsrc(const void* p, unsigned bytes) { return __builtin_amdgcn_make_buffer_rsrc((void*)p, 0, bytes, 0x00020000); }
DI void st16_sc1(rsrc_t r, unsigned voff, unsigned soff, u32x4 v) {
  __builtin_amdgcn_raw_buffer_store_b128(v, r, voff, soff, 16);
  asm volatile("s_nop 1" ::"v"(v) : "memory");
}
DI void st8_sc1(rsrc_t r, unsigned voff, unsigned soff, u32x2 v) { __builtin_amdgcn_raw_buffer_store_b64(v, r, voff, soff, 16); }
DI u32x4 ld16(rsrc_t r, unsigned voff, unsigned soff) { return __builtin_amdgcn_raw_buffer_load_b128(r, voff, soff, 0); }
DI bf16x8 ldfrag(rsrc_t r, unsigned voff, unsigned soff) { return __builtin_bit_cast(bf16x8, ld16(r, voff, soff)); }
DI f32x4 ldf4(rsrc_t r, unsigned voff, unsigned soff) { return __builtin_bit_cast(f32x4, ld16(r, voff, soff)); }
DI u32x4 ld16_sc1(rsrc_t r, unsigned voff, unsigned soff) { return __builtin_amdgcn_raw_buffer_load_b128(r, voff, soff, 16); }
DI u32x2 ld8_sc1(rsrc_t r, unsigned voff, unsigned soff) { return __builtin_amdgcn_raw_buffer_load_b64(r, voff, soff, 16); }

constexpr unsigned SPIN_CAP = 1u << 15;
DI void wait_flags(const unsigned* f, int n, unsigned need, int lane) {
  const GAS unsigned* p = (const GAS unsigned*)f + (lane < n ? lane : 0);
#pragma nounroll
  for (unsigned spin = 0; spin < SPIN_CAP; ++spin) {
    const unsigned v = __hip_atomic_load(p, RLX_AGENT);
    if (__all(v >= need)) break;
    __builtin_amdgcn_s_sleep(1);
  }
  asm volatile("" ::: "memory");
}
DI void set_flag(unsigned* f, unsigned v) { __hip_atomic_store((GAS unsigned*)f, v, RLX_AGENT); }

constexpr size_t WS_MODF = 0;
constexpr size_t WS_MODB = 65536;
constexpr size_t WS_N1 = 60u << 20;
constexpr size_t WS_FLAGS = 131072;
constexpr size_t WS_OF = 1u << 20;
constexpr size_t WS_H2F = 4u << 20;
constexpr size_t WS_WQF = 8u << 20;
constexpr size_t WS_WOF = 12u << 20;
constexpr size_t WS_HF = 16u << 20;
constexpr size_t WS_PART = 40u << 20;
constexpr int FL_O = 0;
constexpr int FL_H2 = 2048;
constexpr int FL_H = 4096;
constexpr int FL_P = FL_H + 8192;
constexpr int FL_ST = FL_P + 4096;
constexpr int FLAG_WORDS = FL_ST + 16384;

namespace k0 {
constexpr int B_PQ = 112, B_STAT = 184, B_PO = 216, B_CODE = 240, B_FLAG = 248, NB = 256;
}
DI void k_pre_body(const u16* __restrict__ x, const u16* __restrict__ wqkv, const u16* __restrict__ wo,
                   const u16* __restrict__ cvec, const u16* __restrict__ wmod, char* __restrict__ ws) {
  using namespace k0;
  __shared__ __attribute__((aligned(16))) char sm[8 * 32 * 160];
  const int tid = threadIdx.x, lane = tid & 63, wave = __builtin_amdgcn_readfirstlane(tid >> 6);
  const int b = blockIdx.x;
  if ((b >= B_PQ && b < B_STAT) || (b >= B_PO && b < B_CODE)) {
    const bool isq = b < B_STAT;
    const int t = isq ? ((b - B_PQ) >> 3) * 8 + wave : (b - B_PO) * 8 + wave;
    if (!isq && t >= 162) return;
    const int kap = t % 18, grp = t / 18, hg = b & 7;
    char* img = sm + wave * (32 * 160);
    const int ch = lane & 7;
    const u16* src;
    if (isq) {
      int c8 = grp * 8 + ch; if (c8 > 26) c8 = 26;
      src = wqkv + (size_t)(kap * 32 + (lane >> 3)) * 1728 + (c8 / 9) * 576 + hg * 72 + (c8 % 9) * 8;
    } else {
      src = wo + (size_t)(kap * 32 + (lane >> 3)) * D_ + grp * 64 + ch * 8;
    }
    const int pitch8 = isq ? 8 * 1728 : 8 * D_;
    u32x4 v[4];
#pragma unroll
    for (int i = 0; i < 4; ++i) v[i] = *(const u32x4*)(src + i * pitch8);
#pragma unroll
    for (int i = 0; i < 4; ++i) *(u32x4*)(img + (i * 8 + (lane >> 3)) * 160 + ch * 16) = v[i];
    asm volatile("s_waitcnt lgkmcnt(0)" ::: "memory");
    const unsigned a0 = (unsigned)(uintptr_t)(LAS char*)img + (8 * (lane >> 4) + ((lane & 15) >> 2)) * 160 + 8 * (lane & 3);
    const rsrc_t dr = mk_rsrc(isq ? ws + WS_WQF + (size_t)(hg * 14 * 18) * 1024 : ws + WS_WOF, 36 * 18 * 1024);
#pragma unroll
    for (int f = 0; f < 4; ++f) {
      const bf16x8 fr = tr_frag(a0 + f * 32, a0 + f * 32 + 4 * 160);
      const int nf = grp * 4 + f;
      if (!isq || nf < 14) __builtin_amdgcn_raw_buffer_store_b128(__builtin_bit_cast(u32x4, fr), dr, (nf * 18 + kap) * 1024 + lane * 16, 0, 0);
    }
    return;
  }
  if (b < B_PQ) {
    const int j = b, cg = ((j >> 4) * 8 + (j & 7)) * 2 + ((j >> 3) & 1);
    if (cg >= 108) return;
    const int rg = tid >> 2, cl = tid & 3;
    const rsrc_t wr = mk_rsrc(wmod, 576 * 3456 * 2), cr = mk_rsrc(cvec, 576 * 2);
    u32x4 w[5]; unsigned cv[5];
#pragma unroll
    for (int i = 0; i < 5; ++i) {
      w[i] = ld16(wr, (unsigned)(rg * 3456 + cl * 8) * 2u, (unsigned)(i * 128 * 3456 + cg * 32) * 2u);
      cv[i] = __builtin_amdgcn_raw_buffer_load_b16(cr, rg * 2, i * 256, 0);
    }
    float acc[8] = {};
#pragma unroll
    for (int i = 0; i < 5; ++i) {
      const float s = rbf(silu_f(bflo(cv[i])));
#pragma unroll
      for (int q = 0; q < 4; ++q) { acc[2 * q] += s * bflo(w[i][q]); acc[2 * q + 1] += s * bfhi(w[i][q]); }
    }
#pragma unroll
    for (int q = 0; q < 8; ++q) {
      acc[q] += __int_as_float(__builtin_amdgcn_update_dpp(0, __float_as_int(acc[q]), 0x124, 0xf, 0xf, false));
      acc[q] += __int_as_float(__builtin_amdgcn_update_dpp(0, __float_as_int(acc[q]), 0x128, 0xf, 0xf, false));
    }
    float* red = (float*)sm;
    if ((lane & 15) < 4) {
      *(f32x4*)(red + (tid >> 4) * 36 + cl * 8) = (f32x4){acc[0], acc[1], acc[2], acc[3]};
      *(f32x4*)(red + (tid >> 4) * 36 + cl * 8 + 4) = (f32x4){acc[4], acc[5], acc[6], acc[7]};
    }
    __syncthreads();
    if (tid < 32) {
      float v = 0.f;
#pragma unroll
      for (int q = 0; q < 32; ++q) v += red[q * 36 + tid];
      v = rbf(v);
      const int vec = cg / 18;
      if (vec == 1 || vec == 4) v = rbf(1.f + v);
      ((float*)(ws + WS_MODF))[cg * 32 + tid] = v;
      if (vec < 2) ((u16*)(ws + WS_MODB))[((cg - vec * 18) * 32 + tid) * 2 + vec] = (u16)(__float_as_uint(v) >> 16);
    }
    return;
  }
  if (b < B_PO) {
    const int row = (b & 7) * 256 + (((b - B_STAT) >> 3) * 8 + wave) * 8 + (lane >> 3), cl = lane & 7;
    u32x4 v[9];
#pragma unroll
    for (int i = 0; i < 9; ++i) v[i] = *(const u32x4*)(x + (size_t)row * D_ + (cl + 8 * i) * 8);
    float s = 0.f;
#pragma unroll
    for (int i = 0; i < 9; ++i)
#pragma unroll
      for (int j = 0; j < 4; ++j) s += bflo(v[i][j]) + bfhi(v[i][j]);
    s += __shfl_xor(s, 1); s += __shfl_xor(s, 2); s += __shfl_xor(s, 4);
    const float mean = s * (1.f / D_);
    float q = 0.f;
#pragma unroll
    for (int i = 0; i < 9; ++i)
#pragma unroll
      for (int j = 0; j < 4; ++j) { const float a = bflo(v[i][j]) - mean, c = bfhi(v[i][j]) - mean; q += a * a + c * c; }
    q += __shfl_xor(q, 1); q += __shfl_xor(q, 2); q += __shfl_xor(q, 4);
    const float mb = rbf(mean), rstd = inv_std(q * (1.f / D_));
#pragma unroll
    for (int i = 0; i < 9; ++i) {
      u32x4 o;
#pragma unroll
      for (int j = 0; j < 4; ++j) {
        const unsigned d = pack_bf16(bflo(v[i][j]) - mb, bfhi(v[i][j]) - mb);
        o[j] = pack_bf16(bflo(d) * rstd, bfhi(d) * rstd);
      }
      *(u32x4*)(ws + WS_N1 + ((size_t)row * D_ + (cl + 8 * i) * 8) * 2) = o;
    }
    return;
  }
  if (b < B_FLAG) {
    unsigned clo, chi, elo, ehi;
    asm volatile("s_getpc_b64 s[20:21]\n\ts_add_u32 %0, s20, k_main@rel32@lo+4\n\ts_addc_u32 %1, s21, k_main@rel32@hi+12"
                 : "=s"(clo), "=s"(chi)::"s20", "s21", "scc");
    asm volatile("s_getpc_b64 s[20:21]\n\ts_add_u32 %0, s20, k_tail@rel32@lo+4\n\ts_addc_u32 %1, s21, k_tail@rel32@hi+12"
                 : "=s"(elo), "=s"(ehi)::"s20", "s21", "scc");
    const GAS char* code = (const GAS char*)(((u64)chi << 32) | clo);
    const long long len = (long long)(((u64)ehi << 32) | elo) - (long long)(((u64)chi << 32) | clo);
    unsigned acc = 0;
#pragma unroll
    for (int i = 0; i < 4; ++i) {
      const int o = (i * 512 + tid) * 64;
      if (o + 4 <= len) acc += *(const GAS unsigned*)(code + o);
    }
    asm volatile("" ::"v"(acc));
    return;
  }
  u32x4* fl = (u32x4*)(ws + WS_FLAGS);
  for (int i = (b - B_FLAG) * 512 + tid; i < FLAG_WORDS / 4; i += 8 * 512) fl[i] = (u32x4){0u, 0u, 0u, 0u};
}

__global__ __launch_bounds__(512) void k_pre(const u16* __restrict__ x, const u16* __restrict__ wqkv, const u16* __restrict__ wo,
                                             const u16* __restrict__ cvec, const u16* __restrict__ wmod, char* __restrict__ ws) {
  k_pre_body(x, wqkv, wo, cvec, wmod, ws);
}

namespace mlp {
constexpr int NBUF = 8, SLOT = 18432, PF = 3, NPRE = 5;
constexpr int RING_BYTES = NBUF * SLOT;
constexpr int OWN = 1 * SLOT;
constexpr int NU1 = 21, NU2 = 9, NU = NU1 + NU2;
constexpr int NS1 = 324, NS2 = 162, NS = NS1 + NS2;
enum { OP_NONE = 0, OP_W, OP_B, OP_POLL, OP_PF };
constexpr int TA_COST = 28;
constexpr int CHK_DELAY = 16;
constexpr int slot_of(int U) { int u = U % NBUF; return u < 3 ? 5 + u : u < 5 ? u : u - 5; }
constexpr int unit_chunk(int U) { return U < 9 ? 0 : U < 15 ? 1 : U < 21 ? 2 : 3; }
constexpr int unit_np(int U) { return unit_chunk(U) == 2 ? 12 : 18; }
constexpr int unit_nk(int U) { int c = unit_chunk(U); return (c == 0 || c == 3) ? 2 : 3; }
constexpr int unit_k0(int U) { return U < 9 ? 2 * U : U < 15 ? 3 * (U - 9) : U < 21 ? 3 * (U - 15) : 2 * (U - 21); }
constexpr int chunk_nt(int c) { return c == 0 ? 4 : c == 1 ? 3 : 2; }
constexpr int chunk_t0(int c) { return c == 0 ? 0 : c == 1 ? 4 : 7; }
constexpr int tile_set(int i) { return i < 4 ? i : i < 7 ? i - 4 : i == 7 ? 3 : 0; }
constexpr int run_a0(int l) { return l == 0 ? 0 : l == 1 ? 3 : l == 2 ? 2 : 5; }
constexpr int run_c1(int l) { return l == 0 ? 4 : l == 1 ? 0 : l == 2 ? 6 : 0; }
constexpr int run_e0(int l) { return l == 0 ? 7 : l == 1 ? 7 : l == 2 ? 0 : 3; }
constexpr int pos_row(int pos) {
  if (pos < 16) return (pos >> 2) * 144 + 16 * (run_a0(pos >> 2) + (pos & 3));
  if (pos < 28) return ((pos - 16) / 3) * 144 + 16 * (run_c1((pos - 16) / 3) + (pos - 16) % 3);
  return ((pos - 28) >> 1) * 144 + 16 * (run_e0((pos - 28) >> 1) + ((pos - 28) & 1));
}
struct Sched {
  short unit[NS], ub[NS], kl[NS], bi[NS], sub[NS], tile[NS], first[NS];
  short blk_u0[NS], blk_nu[NS], bar_wait[NS];
  short op_t[NS], op_a[NS], op_b[NS];
  short pro_n, pro_a[16], pro_b[16];
  short bwait[NS];
  short eA[NS], eB[NS], eC[NS];
  short pub[NS], pub_wait[NS], chk[NS], chk_wait[NS];
  short pst[NS];
  bool ok;
  short min_lead_b, min_lead_w;
};
constexpr Sched make_sched() {
  Sched s{};
  s.ok = true; s.min_lead_b = 1000; s.min_lead_w = 1000;
  int G = 0;
  int blocks[26][2] = {{0,1},{1,1},{2,1},{3,1},{4,1},{5,1},{6,1},{7,2},{9,2},{11,1},{12,1},{13,2},{15,2},{17,1},{18,1},{19,2},
                       {21,1},{22,1},{23,1},{24,1},{25,1},{26,1},{27,3},{-1,0}};
  int blk_of_unit[NU] = {};
  for (int i = 0; i < NS; ++i) { s.blk_u0[i] = -1; s.bar_wait[i] = -1; s.bwait[i] = -1; s.eA[i] = -1; s.eB[i] = -1; s.eC[i] = -1; s.pst[i] = -1; }
  for (int B = 0; blocks[B][0] >= 0; ++B) {
    int u0 = blocks[B][0], nu = blocks[B][1], c = unit_chunk(u0);
    s.blk_u0[G] = (short)u0; s.blk_nu[G] = (short)nu;
    for (int k = 0; k < nu; ++k) blk_of_unit[u0 + k] = B;
    if (c < 3) {
      for (int t = 0; t < chunk_nt(c); ++t)
        for (int k = 0; k < nu; ++k)
          for (int kl = 0; kl < unit_nk(u0); ++kl)
            for (int m = 0; m < 2; ++m) {
              s.unit[G] = (short)(u0 + k); s.ub[G] = (short)k; s.kl[G] = (short)kl; s.bi[G] = (short)(unit_k0(u0 + k) + kl);
              s.sub[G] = (short)m; s.tile[G] = (short)(chunk_t0(c) + t); s.first[G] = (short)(s.bi[G] == 0 && m == 0); ++G;
            }
    } else if (nu == 1) {
      for (int kl = 0; kl < 2; ++kl)
        for (int nf = 0; nf < 9; ++nf) {
          s.unit[G] = (short)u0; s.ub[G] = 0; s.kl[G] = (short)kl; s.bi[G] = (short)(unit_k0(u0) + kl);
          s.tile[G] = (short)nf; s.first[G] = (short)(s.bi[G] == 0); ++G;
        }
    } else {
      for (int nf = 0; nf < 9; ++nf)
        for (int k = 0; k < nu; ++k)
          for (int kl = 0; kl < 2; ++kl) {
            s.unit[G] = (short)(u0 + k); s.ub[G] = (short)k; s.kl[G] = (short)kl; s.bi[G] = (short)(unit_k0(u0 + k) + kl);
            s.tile[G] = (short)nf; ++G;
            if (nf > 0 && k == 0 && kl == 1) s.pst[G - 1] = (short)(nf - 1);
          }
    }
  }
  int store_at[NS] = {}, last_store[3] = {};
  int prev = 0;
  for (int i = 0; i < 9; ++i) {
    int R = 0, D = 100000;
    for (int g = 0; g < NS1; ++g) if (s.tile[g] == i) R = g + 1;
    for (int g = R; g < NS1; ++g) if (tile_set(s.tile[g]) == tile_set(i)) { D = g; break; }
    int st = R > prev ? R : prev;
    int sp = (st + 14 < D) ? 2 : 1;
    if (st + sp * 7 >= D) s.ok = false;
    for (int j = 0; j < 8; ++j) {
      int g = st + sp * j;
      s.eA[g] = (short)(i * 8 + j); s.eB[g + 1] = (short)(i * 8 + j); s.eC[g + 2] = (short)(i * 8 + j);
      if (j == 7) { store_at[g + 2] += 1; last_store[i < 4 ? 0 : i < 7 ? 1 : 2] = g + 2; }
    }
    prev = st + sp * 8;
  }
  const int poll_at[3] = {236, 326, 388};
  for (int c = 0; c < 3; ++c) s.pub[last_store[c] + (c == 2 ? 10 : 14)] = (short)(c + 1);
  int xlast[18] = {};
  for (int g = 0; g < NS1; ++g) xlast[s.bi[g]] = g;
  int ba[96] = {}, bb[96] = {}, be[96] = {}, nb = 0;
  for (int k = 0; k < 18; ++k) for (int m = 0; m < 2; ++m) { ba[nb] = k; bb[nb] = m; be[nb] = 0; ++nb; }
  for (int c = 0; c < 3; ++c) {
    int k0 = c == 0 ? 0 : c == 1 ? 8 : 14, k1 = c == 0 ? 8 : c == 1 ? 14 : 18;
    for (int k = k0; k < k1; ++k) for (int m = 0; m < 2; ++m) {
      int e = poll_at[c] + CHK_DELAY + 1; if (xlast[k] + 1 > e) e = xlast[k] + 1;
      ba[nb] = 18 + k; bb[nb] = m; be[nb] = e; ++nb;
    }
  }
  int wa[96] = {}, wb[96] = {}, nw = 0;
  for (int U = NPRE; U < NU; ++U) for (int j = 0; j < (unit_np(U) + 7) / 8; ++j) { wa[nw] = U; wb[nw] = j; ++nw; }
  int bi = 0, wi = 0;
  while (bi < 8) { s.pro_a[s.pro_n] = (short)ba[bi]; s.pro_b[s.pro_n] = (short)bb[bi]; ++s.pro_n; ++bi; }
  int wneed[NU] = {}, bneed[36] = {};
  for (int g = NS - 1; g >= 0; --g) {
    bneed[(g >= NS1 ? 18 : 0) + s.bi[g]] = g;
    if (s.blk_u0[g] >= 0) for (int k = 0; k < s.blk_nu[g]; ++k) wneed[s.blk_u0[g] + k] = g - PF;
  }
  {
    int bars_done = 0, ta_free = 0;
    for (int g = 0; g < NS; ++g) {
      int gn = g + PF;
      if (gn < NS && s.blk_u0[gn] >= 0) bars_done = blk_of_unit[s.blk_u0[gn]];
      bool polled = false;
      for (int c = 0; c < 3; ++c) if (poll_at[c] == g) { s.op_t[g] = OP_POLL; s.op_a[g] = (short)c; s.chk[g + CHK_DELAY] = (short)(c + 1); polled = true; }
      for (int c = 0; c < 3; ++c) if (g == 151 + 3 * c) { s.op_t[g] = OP_PF; s.op_a[g] = (short)c; polled = true; }
      if (polled || ta_free > g * 10 + 5) continue;
      const int base = ta_free > g * 10 - 5 ? ta_free : g * 10 - 5;
      bool wready = wi < nw && !(wa[wi] - NBUF >= 0 && blk_of_unit[wa[wi] - NBUF] > bars_done - 2);
      bool bready = bi < nb && g >= be[bi];
      int bdl = bready ? bneed[ba[bi]] : 1000000;
      int wdl = wready ? wneed[wa[wi]] - 12 : 1000000;
      if (bready && bdl <= wdl) {
        s.op_t[g] = OP_B; s.op_a[g] = (short)ba[bi]; s.op_b[g] = (short)bb[bi];
        ta_free = base + 8 * TA_COST / 10;
        if (bneed[ba[bi]] - g < s.min_lead_b) s.min_lead_b = (short)(bneed[ba[bi]] - g);
        ++bi;
      } else if (wready) {
        s.op_t[g] = OP_W; s.op_a[g] = (short)wa[wi]; s.op_b[g] = (short)wb[wi];
        const int c = unit_chunk(wa[wi]);
        int pieces = unit_np(wa[wi]) - 8 * wb[wi]; if (pieces > 8) pieces = 8;
        ta_free = base + (c == 0 ? 8 : c == 3 ? 11 : c == 1 ? 14 : 20) * TA_COST * pieces / 80;
        if (wneed[wa[wi]] - g < s.min_lead_w) s.min_lead_w = (short)(wneed[wa[wi]] - g);
        ++wi;
      }
    }
    if (bi < nb || wi < nw || s.min_lead_b <= 2 || s.min_lead_w <= 2) s.ok = false;
  }
  {
    int n = 0, wpos[NU] = {}, bpos[36] = {}, pollpos = 0, storepos = 0;
    bool bseen[36] = {};
    for (int i = 0; i < s.pro_n; ++i) { ++n; bpos[s.pro_a[i]] = n; }
    s.bar_wait[0] = (short)n;
    for (int g = 0; g < NS; ++g) {
      int gn = g + PF;
      if (gn < NS && s.blk_u0[gn] >= 0) {
        int need = 0;
        for (int k = 0; k < s.blk_nu[gn]; ++k) if (wpos[s.blk_u0[gn] + k] > need) need = wpos[s.blk_u0[gn] + k];
        s.bar_wait[gn] = (short)(n - need);
      }
      if (s.op_t[g] == OP_POLL) { ++n; pollpos = n; }
      if (s.op_t[g] == OP_PF) ++n;
      if (s.op_t[g] == OP_B) { ++n; bpos[s.op_a[g]] = n; }
      if (s.op_t[g] == OP_W) { if ((s.op_b[g] + 1) * 8 <= unit_np(s.op_a[g])) ++n; wpos[s.op_a[g]] = n; }
      if (s.chk[g]) s.chk_wait[g] = (short)(n - pollpos);
      int bk = (g >= NS1 ? 18 : 0) + s.bi[g];
      if (!bseen[bk]) { bseen[bk] = true; s.bwait[g] = (short)(n - bpos[bk]); }
      if (store_at[g]) { n += store_at[g]; storepos = n; }
      if (s.pub[g]) { s.pub_wait[g] = (short)(n - storepos); ++n; }
    }
  }
  for (int g = 0; g < NS; ++g) if (s.bar_wait[g] > 63 || s.bwait[g] > 63 || s.pub_wait[g] > 63 || s.chk_wait[g] > 63) s.ok = false;
  return s;
}
constexpr Sched SC = make_sched();
static_assert(SC.ok, "schedule");
}

namespace p1 {
constexpr int XS = 0;
constexpr int QP = 456;
constexpr int OS = 40960;
constexpr int OP = 160;
}
namespace p2 {
constexpr int OSM = 0;
constexpr int RED = 36864;
constexpr int RST = RED + 32 * 37 * 8;
constexpr int X1S = mlp::RING_BYTES;
constexpr int MV = X1S + 9216;
}
constexpr int SCRATCH = p2::MV + 4 * 144 * 4;
constexpr int LDS_BYTES = SCRATCH + 1024;

DI void mem_fence() { asm volatile("" ::: "memory"); }

template <int NN, int M0>
DI void p1_gemm(char* smem, bf16x8 (&wf)[2][18], rsrc_t wr, unsigned wso, rsrc_t wor, unsigned wo0, unsigned wo8, f32x4 (&acc)[2][5], int lane) {
  const int g = lane >> 4, m16 = lane & 15;
  const char* xs0 = smem + p1::XS + (g * 16 + (m16 ^ (2 * g))) * 16;
  const char* xs1 = smem + p1::XS + (g * 16 + (m16 ^ (2 * g + 1))) * 16;
  static_for<18>([&](auto kc) LAMBDA_INLINE {
    constexpr int k = decltype(kc)::value;
    if constexpr (k + 6 < 18) {
#pragma unroll
      for (int n = 0; n < NN; ++n) wf[n][k + 6] = ldfrag(wr, lane * 16, wso + (n * 18 + k + 6) * 1024);
    }
    if constexpr (NN == 1) wf[1][k] = ldfrag(wor, lane * 16, wo8 + k * 1024);
    if constexpr (k >= 12) {
      wf[0][2 * (k - 12)] = ldfrag(wor, lane * 16, wo0 + 2 * (k - 12) * 1024);
      wf[0][2 * (k - 12) + 1] = ldfrag(wor, lane * 16, wo0 + (2 * (k - 12) + 1) * 1024);
    }
    bf16x8 bf[5];
#pragma unroll
    for (int m = M0; m < 5; ++m) bf[m] = *(const bf16x8*)(((k & 1) ? xs1 : xs0) + (k * 5 + m) * 1024);
#pragma unroll
    for (int n = 0; n < NN; ++n)
#pragma unroll
      for (int m = M0; m < 5; ++m) acc[n][m] = __builtin_amdgcn_mfma_f32_16x16x32_bf16(wf[n][k], bf[m], acc[n][m], 0, 0, 0);
    mem_fence();
  });
}

extern "C" __global__ __launch_bounds__(512, 2) void k_main(const u16* __restrict__ x, const u16* __restrict__ w1, const u16* __restrict__ w3,
                                                 const u16* __restrict__ w2, char* __restrict__ ws, u16* __restrict__ out) {
  using namespace mlp;
  extern __shared__ __attribute__((aligned(16))) char smem[];
  const int tid = threadIdx.x, lane = tid & 63;
  const int wave = __builtin_amdgcn_readfirstlane(tid >> 6);
  const int b = blockIdx.x, xcd = b & 7, idx = b >> 3;
  const int mt = idx >> 2, ftl = idx & 3;
  const unsigned lds0 = (unsigned)(uintptr_t)(LAS char*)smem;
  unsigned* flags = (unsigned*)(ws + WS_FLAGS);
  const int a0 = ftl == 0 ? 0 : ftl == 1 ? 3 : ftl == 2 ? 2 : 5;
  const int c1 = ftl == 0 ? 4 : ftl == 1 ? 0 : ftl == 2 ? 6 : 0;
  const int e0 = ftl == 0 ? 7 : ftl == 1 ? 7 : ftl == 2 ? 0 : 3;
  const int fbase = (xcd * 4 + ftl) * 144;

  const char* sp[3];
  unsigned so[3];
  auto stage_setup = [&](auto cc_) LAMBDA_INLINE {
    constexpr int c = decltype(cc_)::value;
    const int lane = lane_now();
#pragma unroll
    for (int j = 0; j < (c == 2 ? 2 : 3); ++j) {
      int i = wave + 8 * j;
      if (c == 2) { if (i > 11) i = 11; } else { if (i > 17) i = 17; }
      const int ci = i * 64 + lane;
      if constexpr (c == 0) {
        int row = ci / 18, cc = ci % 18; if (cc > 15) cc = 15;
        sp[j] = (const char*)((cc >= 8 ? w3 : w1) + (size_t)row * F_ + fbase + a0 * 16 + (cc & 7) * 8);
      } else if constexpr (c == 1) {
        int row = ci / 12, cc = ci % 12;
        sp[j] = (const char*)((cc >= 6 ? w3 : w1) + (size_t)row * F_ + fbase + c1 * 16 + (cc >= 6 ? cc - 6 : cc) * 8);
      } else if constexpr (c == 2) {
        int row = ci >> 3, cc = ci & 7, pos = (cc >> 1) ^ ((row >> 1) & 1);
        sp[j] = (const char*)((pos >= 2 ? w3 : w1) + (size_t)row * F_ + fbase + e0 * 16 + (pos & 1) * 16 + (cc & 1) * 8);
      } else {
        int ip = i >= 9 ? i - 9 : i, c2 = ip * 64 + lane, row = c2 / 18, cc = c2 % 18;
        so[j] = (unsigned)(row * D_ + cc * 8) * 2u;
      }
    }
  };
  const u16* w2b = w2 + (size_t)xcd * 576 * D_ + ftl * 144;
  auto stage_round = [&](auto Uc, auto jc) LAMBDA_INLINE {
    constexpr int U = decltype(Uc)::value, j = decltype(jc)::value, c = unit_chunk(U);
    if constexpr (j == 0 && unit_k0(U) == 0) stage_setup(IC(c));
    const unsigned dst = lds0 + slot_of(U) * SLOT + (wave + 8 * j) * 1024;
    constexpr bool all = (j + 1) * 8 <= unit_np(U);
    if (all || wave + 8 * j < unit_np(U)) {
      if constexpr (c < 3) {
        glds16v(sp[j], dst);
      } else {
        constexpr int pA = 2 * unit_k0(U), pB = pA + 2;
        const bool h1 = wave + 8 * j >= 9;
        const int r0 = h1 ? pos_row(pB) : pos_row(pA);
        constexpr int dA = pos_row(pA + 1) - pos_row(pA) - 16, dB = pos_row(pB + 1) - pos_row(pB) - 16;
        unsigned vo = so[j];
        if constexpr (dA != 0 || dB != 0) vo += (so[j] >= 16u * D_ * 2u) ? (unsigned)((h1 ? dB : dA) * D_ * 2) : 0u;
        glds16(vo, w2b + (size_t)r0 * D_, dst);
      }
    }
    if constexpr (c < 3) sp[j] += (size_t)unit_nk(U) * 32 * F_ * 2;
  };

  const int g = lane >> 4, m16 = lane & 15;
  bf16x8 wf[2][18];
  {
    using namespace p1;
    const int rt = xcd * 4 + (idx >> 3), hg = idx & 7;
    const int r0 = rt * 64 - 16;
    const rsrc_t xr = mk_rsrc(ws + WS_N1, S_ * D_ * 2), mr = mk_rsrc(ws + WS_MODF, 65536 + 576 * 4);
    const rsrc_t wr = mk_rsrc(ws + WS_WQF, 8 * 14 * 18 * 1024);
    const int r8 = lane >> 3, ch = lane & 7;
    u32x4 sa[2][2];
#pragma unroll
    for (int part = 0; part < 2; ++part) {
      const unsigned so_ = 65536 + (part ? 8 : wave) * 256;
      sa[part][0] = ld16(mr, ch * 32, so_); sa[part][1] = ld16(mr, ch * 32 + 16, so_);
    }
    u32x4 xv[12];
#pragma unroll
    for (int i = 0; i < 12; ++i) {
      const int rg = i < 10 ? i : i == 10 ? wave : 8 + (wave & 1), ln = i < 10 ? wave : 8;
      if (i < 11 || wave >= 6) xv[i] = ld16(xr, r8 * 1152 + ch * 16, (unsigned)((r0 + rg * 8) * 1152 + ln * 128));
    }
    f32x4 mvv;
    if (wave < 3) mvv = ldf4(mr, tid < 144 ? ((2 + tid / 36) * D_ + (tid % 36) * 4) * 4 : 0x40000000, ftl * 576);
    barrier_raw();
    const int nf0 = wave < 4 ? 4 + 2 * wave : wave < 6 ? 2 * (wave - 4) : 6 + wave;
    const unsigned wso = (unsigned)((hg * 14 + nf0) * 18 * 1024);
    auto w_load = [&](int k) LAMBDA_INLINE {
      wf[0][k] = ldfrag(wr, lane * 16, wso + k * 1024);
      if (wave < 6) wf[1][k] = ldfrag(wr, lane * 16, wso + (18 + k) * 1024);
      mem_fence();
    };
    w_load(0); w_load(1);
    {
      const int gq = ch & 3, kb = ch >> 2, swz = 2 * gq + kb;
      unsigned am[2][8]; float sm8[2][8];
#pragma unroll
      for (int part = 0; part < 2; ++part)
#pragma unroll
        for (int j = 0; j < 8; ++j) {
          const unsigned m = sa[part][j >> 2][j & 3];
          am[part][j] = (j & 1) ? (m & 0xffff0000u) : (m >> 16);
          sm8[part][j] = bflo(m);
        }
#pragma unroll
      for (int i = 0; i < 12; ++i) {
        const int part = i >= 10, ln = part ? 8 : wave;
        const int rg = i < 10 ? i : i == 10 ? wave : 8 + (wave & 1);
        if (i < 11 || wave >= 6) {
          u32x4 o;
          float t8[8];
#pragma unroll
          for (int j = 0; j < 8; ++j) t8[j] = dot2v(xv[i][j >> 1], am[part][j], 0.f);
#pragma unroll
          for (int j = 0; j < 4; ++j) o[j] = cvt2v(t8[2 * j], t8[2 * j + 1]);
#pragma unroll
          for (int j = 0; j < 8; ++j) t8[j] = dot2v(o[j >> 1], (j & 1) ? 0x3f800000u : 0x00003f80u, sm8[part][j]);
#pragma unroll
          for (int j = 0; j < 4; ++j) o[j] = cvt2v(t8[2 * j], t8[2 * j + 1]);
          *(u32x4*)(smem + XS + ((2 * ln + kb) * 5 + (rg >> 1)) * 1024 + gq * 256 + (((rg & 1) * 8 + r8) ^ swz) * 16) = o;
        }
        if (i < 8 && (i & 1)) w_load(2 + (i >> 1));
      }
    }
    if (tid < 144) *(f32x4*)(smem + p2::MV + tid * 16) = mvv;
    lds_barrier();
    const rsrc_t wor = mk_rsrc(ws + WS_WOF, 36 * 18 * 1024);
    const unsigned wo0 = (unsigned)((ftl * 9 + wave) * 18 * 1024), wo8 = (unsigned)((ftl * 9 + 8) * 18 * 1024);
    f32x4 acc[2][5] = {};
    if (wave < 4) p1_gemm<2, 0>(smem, wf, wr, wso, wor, wo0, wo8, acc, lane);
    else if (wave < 6) p1_gemm<2, 1>(smem, wf, wr, wso, wor, wo0, wo8, acc, lane);
    else {
      p1_gemm<1, 0>(smem, wf, wr, wso, wor, wo0, wo8, acc, lane);
    }
    lds_barrier();
#pragma unroll
    for (int n = 0; n < 2; ++n)
#pragma unroll
      for (int m = 0; m < 5; ++m) {
        if (n == 1 && wave >= 6) continue;
        u32x2 v = {pack_bf16(acc[n][m][0], acc[n][m][1]), pack_bf16(acc[n][m][2], acc[n][m][3])};
        *(u32x2*)(smem + (m * 16 + m16) * QP + ((nf0 + n) * 16 + 4 * g) * 2) = v;
      }
    lds_barrier();
    auto wo_load = [&](int k) LAMBDA_INLINE { wf[0][k] = ldfrag(wor, lane * 16, wo0 + k * 1024); mem_fence(); };
    {
      typedef __bf16 bf2 __attribute__((ext_vector_type(2)));
      const int rl = (wave >> 1) * 16 + m16, hd = (wave & 1) * 2 + (g & 1), hw = lane >> 5;
      const int grow = rt * 64 + rl;
      const unsigned* qrow = (const unsigned*)(smem + (rl + 16) * QP) + hd * 9;
      unsigned qv[9];
#pragma unroll
      for (int d = 0; d < 9; ++d) qv[d] = qrow[d];
      wo_load(12); wo_load(13);
      float lg[4];
#pragma unroll
      for (int w = 0; w < 4; ++w) {
        const int ww = hw * 4 + w;
        const unsigned* kr = (const unsigned*)(smem + (rl + 16 - 2 * ww) * QP) + 36 + hd * 9;
        float a = 0.f;
#pragma unroll
        for (int d = 0; d < 9; ++d) a = __builtin_amdgcn_fdot2_f32_bf16(__builtin_bit_cast(bf2, qv[d]), __builtin_bit_cast(bf2, kr[d]), a, false);
        a = rbf(rbf(a) * 0.23570226039551584f);
        lg[w] = (grow - 2 * ww >= 0) ? a : -1e9f;
        wo_load(14 + w);
      }
      auto both = [&](float v) LAMBDA_INLINE { return __builtin_amdgcn_permlane32_swap(__float_as_uint(v), __float_as_uint(v), false, false); };
      float l8[8];
#pragma unroll
      for (int w = 0; w < 4; ++w) { const auto r = both(lg[w]); l8[w] = __uint_as_float(r[0]); l8[4 + w] = __uint_as_float(r[1]); }
      const float mxv = fmaxf(fmaxf(fmaxf(l8[0], l8[1]), fmaxf(l8[2], l8[3])), fmaxf(fmaxf(l8[4], l8[5]), fmaxf(l8[6], l8[7])));
      float sum = 0.f;
#pragma unroll
      for (int w = 0; w < 8; ++w) { l8[w] = __builtin_amdgcn_exp2f((l8[w] - mxv) * 1.44269504089f); sum += l8[w]; }
      const float inv = __builtin_amdgcn_rcpf(sum);
      float o[10] = {};
#pragma unroll
      for (int w = 0; w < 8; w += 2) {
        const bf2 pp = __builtin_bit_cast(bf2, pack_bf16(l8[w] * inv, l8[w + 1] * inv));
        const unsigned* v0 = (const unsigned*)(smem + (rl + 16 - 2 * w) * QP) + 72 + hd * 9 + hw * 5;
        const unsigned* v1 = (const unsigned*)(smem + (rl + 14 - 2 * w) * QP) + 72 + hd * 9 + hw * 5;
#pragma unroll
        for (int d = 0; d < 5; ++d) {
          const unsigned a = v0[d], c = v1[d];
          o[2 * d] = __builtin_amdgcn_fdot2_f32_bf16(pp, __builtin_bit_cast(bf2, __builtin_amdgcn_perm(c, a, 0x05040100u)), o[2 * d], false);
          o[2 * d + 1] = __builtin_amdgcn_fdot2_f32_bf16(pp, __builtin_bit_cast(bf2, __builtin_amdgcn_perm(c, a, 0x07060302u)), o[2 * d + 1], false);
        }
      }
      unsigned* orow = (unsigned*)(smem + OS + rl * OP) + hd * 9 + hw * 5;
#pragma unroll
      for (int d = 0; d < 5; ++d) if (d < 4 || hw == 0) orow[d] = pack_bf16(o[2 * d], o[2 * d + 1]);
    }
    lds_barrier();
    const rsrc_t ors = mk_rsrc(ws + WS_OF, S_ * D_ * 2);
#pragma unroll
    for (int it = 0; it < 2; ++it) {
      const int pc = tid + it * 512;
      if (pc < 576) {
        const int row = pc & 63, c = pc >> 6;
        const u32x4 v = *(const u32x4*)(smem + OS + row * OP + c * 16);
        const int c8 = hg * 9 + c, mf = rt * 4 + (row >> 4);
        st16_sc1(ors, (unsigned)(((mf * 18 + (c8 >> 2)) * 64 + (c8 & 3) * 16 + (row & 15)) * 16), 0, v);
      }
    }
    wait_vm<0>();
    barrier_raw();
    if (tid == 0) set_flag(flags + FL_O + rt * 32 + hg, 1u);
  }

  {
    using namespace p2;
    const int rt = mt * 8 + xcd, nt = ftl;
    u32x2 xr[3];
#pragma unroll
    for (int t = 0; t < 3; ++t) {
      const int nf = t < 2 ? wave : 8, mi = (t ^ wave) & 1;
      xr[t] = *(const u32x2*)(x + (size_t)(rt * 32 + mi * 16 + m16) * D_ + nt * 144 + nf * 16 + 4 * g);
    }
    wait_flags(flags + FL_O + (rt >> 1) * 32, 8, 1u, lane);
    const rsrc_t ors = mk_rsrc(ws + WS_OF, S_ * D_ * 2);
    u32x4 ov[5];
#pragma unroll
    for (int i = 0; i < 5; ++i) {
      const int f = wave + 8 * i;
      if (f < 36) ov[i] = ld16_sc1(ors, lane * 16, (unsigned)((rt * 36 + f) * 1024));
    }
#pragma unroll
    for (int i = 0; i < 5; ++i) {
      const int f = wave + 8 * i;
      if (f < 36) *(u32x4*)(smem + OSM + f * 1024 + lane * 16) = ov[i];
    }
    lds_barrier();
    f32x4 acc[3] = {};
    bf16x8 ob[2][18];
    const char* o0 = smem + OSM + (wave & 1) * 18432 + lane * 16;
    const char* o1 = smem + OSM + (1 - (wave & 1)) * 18432 + lane * 16;
#pragma unroll
    for (int k = 0; k < 3; ++k) { ob[0][k] = *(const bf16x8*)(o0 + k * 1024); ob[1][k] = *(const bf16x8*)(o1 + k * 1024); }
    static_for<18>([&](auto kc) LAMBDA_INLINE {
      constexpr int k = decltype(kc)::value;
      if constexpr (k + 3 < 18) {
        ob[0][k + 3] = *(const bf16x8*)(o0 + (k + 3) * 1024);
        ob[1][k + 3] = *(const bf16x8*)(o1 + (k + 3) * 1024);
      }
      acc[0] = __builtin_amdgcn_mfma_f32_16x16x32_bf16(wf[0][k], ob[0][k], acc[0], 0, 0, 0);
      acc[1] = __builtin_amdgcn_mfma_f32_16x16x32_bf16(wf[0][k], ob[1][k], acc[1], 0, 0, 0);
      if (wave >= 6) acc[2] = __builtin_amdgcn_mfma_f32_16x16x32_bf16(wf[1][k], ob[0][k], acc[2], 0, 0, 0);
      if constexpr (k < 3 * NPRE) stage_round(IC(k / 3), IC(k % 3));
    });
    float2* red = (float2*)(smem + RED);
    const float* mv = (const float*)(smem + MV);
    f32x4 xv[3];
#pragma unroll
    for (int t = 0; t < 3; ++t) {
      if (t == 2 && wave < 6) continue;
      const int nf = t < 2 ? wave : 8, mi = (t ^ wave) & 1;
      const f32x4 gv = *(const f32x4*)(mv + nf * 16 + 4 * g);
      u32x2 xb = {resid2(xr[t][0], gv[0], gv[1], acc[t][0], acc[t][1]), resid2(xr[t][1], gv[2], gv[3], acc[t][2], acc[t][3])};
      *(u32x2*)(smem + X1S + ((nf * 2 + mi) * 64 + lane) * 8) = xb;
      f32x4 v = {bflo(xb[0]), bfhi(xb[0]), bflo(xb[1]), bfhi(xb[1])};
      xv[t] = v;
      const float mu = (v[0] + v[1] + v[2] + v[3]) * 0.25f;
      const float q = (v[0] - mu) * (v[0] - mu) + (v[1] - mu) * (v[1] - mu) + (v[2] - mu) * (v[2] - mu) + (v[3] - mu) * (v[3] - mu);
      red[(mi * 16 + m16) * 37 + nf * 4 + g] = make_float2(mu, q);
    }
    lds_barrier();
    GAS u64* stg = (GAS u64*)(flags + FL_ST) + (size_t)rt * 128;
    if (wave == 0) {
      const int row = lane & 31, hf = lane >> 5;
      float2 e[18];
#pragma unroll
      for (int i = 0; i < 18; ++i) e[i] = red[row * 37 + hf * 18 + i];
      float mu = 0.f;
#pragma unroll
      for (int i = 0; i < 18; ++i) mu += e[i].x;
      mu += __shfl_xor(mu, 32);
      mu *= (1.f / 36.f);
      float q = 0.f;
#pragma unroll
      for (int i = 0; i < 18; ++i) q += e[i].y + 4.f * (e[i].x - mu) * (e[i].x - mu);
      q += __shfl_xor(q, 32);
      if (lane < 32) __hip_atomic_store(stg + nt * 32 + row, ((u64)(__float_as_uint(q) | 0x80000000u) << 32) | __float_as_uint(mu), RLX_AGENT);
      u64 e0v = 0, e1v = 0;
#pragma nounroll
      for (unsigned spin = 0; spin < SPIN_CAP; ++spin) {
        e0v = __hip_atomic_load(stg + lane, RLX_AGENT);
        e1v = __hip_atomic_load(stg + 64 + lane, RLX_AGENT);
        if (__all((e0v >> 63) && (e1v >> 63))) break;
        __builtin_amdgcn_s_sleep(1);
      }
      const float m0 = __uint_as_float((unsigned)e0v), m1 = __uint_as_float((unsigned)e1v);
      const float q0 = __uint_as_float((unsigned)(e0v >> 32) & 0x7fffffffu), q1 = __uint_as_float((unsigned)(e1v >> 32) & 0x7fffffffu);
      float ms = m0 + m1; ms += __shfl_xor(ms, 32);
      const float mean = 0.25f * ms;
      float qs = q0 + q1 + 144.f * ((m0 - mean) * (m0 - mean) + (m1 - mean) * (m1 - mean));
      qs += __shfl_xor(qs, 32);
      if (lane < 32) ((float2*)(smem + RST))[row] = make_float2(inv_std(qs * (1.f / D_)), rbf(mean));
    }
    lds_barrier();
    const rsrc_t hrs2 = mk_rsrc(ws + WS_H2F, S_ * D_ * 2);
#pragma unroll
    for (int t = 0; t < 3; ++t) {
      if (t == 2 && wave < 6) continue;
      const int nf = t < 2 ? wave : 8, mi = (t ^ wave) & 1;
      const float2 st = ((const float2*)(smem + RST))[mi * 16 + m16];
      const f32x4 sbv = *(const f32x4*)(mv + 144 + nf * 16 + 4 * g), cbv = *(const f32x4*)(mv + 288 + nf * 16 + 4 * g);
      float h[4];
#pragma unroll
      for (int j = 0; j < 4; j += 2) {
        const unsigned d = pack_bf16(xv[t][j] - st.y, xv[t][j + 1] - st.y);
        const unsigned n = pack_bf16(bflo(d) * st.x, bfhi(d) * st.x);
        const unsigned p = pack_bf16(bflo(n) * cbv[j], bfhi(n) * cbv[j + 1]);
        h[j] = bflo(p) + sbv[j]; h[j + 1] = bfhi(p) + sbv[j + 1];
      }
      const int nfl = nf + (nt & 1);
      u32x2 o = {pack_bf16(h[0], h[1]), pack_bf16(h[2], h[3])};
      *(u32x2*)(smem + OSM + (nfl >> 1) * 2048 + (g * 32 + mi * 16 + m16) * 16 + (nfl & 1) * 8) = o;
    }
    lds_barrier();
    {
      const int kap0 = (nt * 9) >> 1, full0 = nt & 1, halfk = (nt & 1) ? 0 : 4;
      const u32x4 v = *(const u32x4*)(smem + OSM + full0 * 2048 + wave * 1024 + lane * 16);
      st16_sc1(hrs2, lane * 16, (unsigned)((rt * 18 + kap0 + full0) * 2048 + wave * 1024), v);
      if (wave < 2) {
        const u32x2 e = *(const u32x2*)(smem + OSM + halfk * 2048 + wave * 1024 + lane * 16 + (nt & 1) * 8);
        st8_sc1(hrs2, lane * 16 + (nt & 1) * 8, (unsigned)((rt * 18 + kap0 + halfk) * 2048 + wave * 1024), e);
      }
    }
    wait_vm<0>();
    barrier_raw();
    if (tid == 0) set_flag(flags + FL_H2 + rt * 32 + nt, 1u);
  }

  bf16x8 xf[18][2];
  const int mf0 = (mt * 8 + wave) * 2;
  const char* xb = ws + WS_H2F + (size_t)(mt * 8 + wave) * 36 * 1024;
  const char* hb = ws + WS_HF + ((size_t)mf0 * 8 + xcd) * 18432;
  const unsigned* hfl = flags + FL_H + ((xcd * 8 + mt) * 8 + wave) * 16;
  const unsigned l16 = lane * 16;
  unsigned flagv;
  auto bload = [&](auto ac, auto mc) LAMBDA_INLINE {
    constexpr int a = decltype(ac)::value, mi = decltype(mc)::value;
    if constexpr (a < 18) {
      constexpr int o = (a * 2 + mi) * 1024;
      gload16_sc1<o % 4096>(xf[a][mi], l16, xb + (o / 4096) * 4096);
    } else {
      constexpr int o = (a - 18) * 1024;
      gload16_sc1<o % 4096>(xf[a - 18][mi], l16, hb + mi * 8 * 18432 + (o / 4096) * 4096);
    }
  };
  wait_flags(flags + FL_H2 + (mt * 8 + wave) * 32, 4, 1u, lane);
  static_for<SC.pro_n>([&](auto ic) LAMBDA_INLINE { constexpr int i = decltype(ic)::value; bload(IC(SC.pro_a[i]), IC(SC.pro_b[i])); });

  unsigned ubase[3][2];
  auto block_barrier = [&](auto Gc) LAMBDA_INLINE {
    constexpr int G = decltype(Gc)::value, u0 = SC.blk_u0[G], nu = SC.blk_nu[G], c = unit_chunk(u0);
    sched_fence();
    wait_vm<SC.bar_wait[G]>();
    barrier_raw();
    const int ln = lane_now(), p = ln & 3, q = (ln >> 2) & 3, blk = (ln >> 4) & 1, hh = ln >> 5;
#pragma unroll
    for (int k = 0; k < nu; ++k) {
      const unsigned sb = lds0 + slot_of(u0 + k) * SLOT + 8 * p;
      if constexpr (c == 0) ubase[k][0] = sb + (4 * hh + q) * 288 + blk * 128;
      else if constexpr (c == 1) ubase[k][0] = sb + (4 * hh + q) * 192 + blk * 96;
      else if constexpr (c == 2) {
        ubase[k][0] = sb + (4 * hh + q) * 128 + blk * 64 + 32 * (q >> 1);
        ubase[k][1] = sb + (4 * hh + q) * 128 + blk * 64 + 32 * (1 - (q >> 1));
      } else ubase[k][0] = sb + (16 * hh + 4 * blk + q) * 288;
      asm volatile("" : "+v"(ubase[k][0]));
      if constexpr (c == 2) asm volatile("" : "+v"(ubase[k][1]));
    }
    sched_fence();
  };
  auto afrag = [&](auto Gc) LAMBDA_INLINE {
    constexpr int G = decltype(Gc)::value, U = SC.unit[G], c = unit_chunk(U), k = SC.ub[G], kl = SC.kl[G], sb = SC.sub[G];
    if constexpr (c == 0) {
      constexpr unsigned o = (kl * 32 + 8 * sb) * 288 + SC.tile[G] * 32;
      return tr_frag(ubase[k][0] + o, ubase[k][0] + o + 16 * 288);
    } else if constexpr (c == 1) {
      constexpr unsigned o = (kl * 32 + 8 * sb) * 192 + (SC.tile[G] - 4) * 32;
      return tr_frag(ubase[k][0] + o, ubase[k][0] + o + 16 * 192);
    } else if constexpr (c == 2) {
      constexpr unsigned o = (kl * 32 + 8 * sb) * 128;
      return tr_frag(ubase[k][SC.tile[G] - 7] + o, ubase[k][SC.tile[G] - 7] + o + 16 * 128);
    } else {
      constexpr unsigned o = kl * 32 * 288 + SC.tile[G] * 32;
      return tr_frag(ubase[k][0] + o, ubase[k][0] + o + 8 * 288);
    }
  };
  f32x16 acc[4];
  f32x4 acc2[9][2];
  bf16x8 af[PF + 1];
  float ta[2], tp[2], hv[2];
  u32x4 pk;
  const rsrc_t hrs = mk_rsrc(ws + WS_HF, 128 * 8 * 18432);
  const rsrc_t prs = mk_rsrc(ws + WS_PART, 64 * 32 * 9 * 1024);
  const unsigned hso = (unsigned)((mf0 * 8 + xcd) * 18432);
  const unsigned tile = (unsigned)(mt * 4 + ftl);
  auto epi = [&](auto Gc) LAMBDA_INLINE {
    constexpr int G = decltype(Gc)::value;
    if constexpr (SC.eC[G] >= 0) {
      constexpr int e = SC.eC[G], i = e >> 3, j = e & 7;
      hv[j & 1] = tp[e & 1] * ta[e & 1];
      if constexpr (j & 1) {
        pk[j >> 1] = pack_bf16(hv[0], hv[1]);
        asm volatile("" : "+v"(pk));
      } else {
        asm volatile("" : "+v"(hv[0]));
      }
      if constexpr (j == 7) {
        const int ln = lane_now();
        const int pos = i < 4 ? 4 * ftl + i : i < 7 ? 16 + 3 * ftl + (i - 4) : 28 + 2 * ftl + (i - 7);
        st16_sc1(hrs, (unsigned)(((ln >> 4) & 1) * 8 * 18432 + (ln >> 5) * 256 + (ln & 15) * 16), hso + pos * 512, pk);
      }
    }
    if constexpr (SC.eB[G] >= 0) {
      constexpr int e = SC.eB[G];
      ta[e & 1] = __builtin_amdgcn_rcpf(1.f + ta[e & 1]);
      asm volatile("" : "+v"(ta[e & 1]));
    }
    if constexpr (SC.eA[G] >= 0) {
      constexpr int e = SC.eA[G], i = e >> 3, j = e & 7, st = tile_set(i);
      const float gv = acc[st][j];
      tp[e & 1] = gv * acc[st][8 + j];
      ta[e & 1] = __builtin_amdgcn_exp2f(gv * -1.44269504089f);
      asm volatile("" : "+v"(ta[e & 1]), "+v"(tp[e & 1]));
    }
  };
  auto part_store = [&](auto nc) LAMBDA_INLINE {
    constexpr int nf = decltype(nc)::value;
    u32x4 v = {pack_bf16(acc2[nf][0][0], acc2[nf][0][1]), pack_bf16(acc2[nf][0][2], acc2[nf][0][3]),
               pack_bf16(acc2[nf][1][0], acc2[nf][1][1]), pack_bf16(acc2[nf][1][2], acc2[nf][1][3])};
    if (wave == xcd) *(u32x4*)(smem + OWN + nf * 1024 + l16) = v;
    else st16_sc1(prs, l16, (((wave * 8 + xcd) * 32 + tile) * 9 + nf) * 1024, v);
  };

  block_barrier(IC(0));
  static_for<PF>([&](auto ic) LAMBDA_INLINE { constexpr int G = decltype(ic)::value; af[G % (PF + 1)] = afrag(IC(G)); });
  static_for<NS>([&](auto ic) LAMBDA_INLINE {
    constexpr int G = decltype(ic)::value, GN = G + PF, kk = SC.bi[G];
    if constexpr (GN < NS) {
      if constexpr (SC.blk_u0[GN] >= 0) block_barrier(IC(GN));
    }
    if constexpr (SC.op_t[G] == OP_W) stage_round(IC(SC.op_a[G]), IC(SC.op_b[G]));
    if constexpr (SC.op_t[G] == OP_B) bload(IC(SC.op_a[G]), IC(SC.op_b[G]));
    if constexpr (SC.op_t[G] == OP_POLL) gload4_sc1(flagv, (lane_now() & 3) * 4, hfl);
    if constexpr (SC.op_t[G] == OP_PF) {
      int pc = SC.op_a[G] * 8 + wave; if (pc > 19) pc = 19;
      glds16(l16, (const char*)w2 + (size_t)(xcd * 576 + idx * 18) * 1152 + pc * 1024 + (pc == 19 && wave > 3 ? 256 : 0), lds0 + SCRATCH);
    }
    if constexpr (SC.chk[G] != 0) {
      asm volatile("s_waitcnt vmcnt(%1)" : "+v"(flagv) : "i"(SC.chk_wait[G]) : "memory");
      if (!__all(flagv >= (unsigned)SC.chk[G])) wait_flags(hfl, 4, (unsigned)SC.chk[G], lane_now());
    }
    if constexpr (SC.bwait[G] >= 0)
      asm volatile("s_waitcnt vmcnt(%2)" : "+v"(xf[kk][0]), "+v"(xf[kk][1]) : "i"(SC.bwait[G]) : "memory");
    if constexpr (G < NS1) {
      constexpr int st = tile_set(SC.tile[G]);
      const f32x16 z = {};
      acc[st] = __builtin_amdgcn_mfma_f32_32x32x16_bf16(af[G % (PF + 1)], xf[kk][SC.sub[G]], SC.first[G] ? z : acc[st], 0, 0, 0);
      af[GN % (PF + 1)] = afrag(IC(GN));
    } else {
      constexpr int nf = SC.tile[G];
      const f32x4 z = {};
      acc2[nf][0] = __builtin_amdgcn_mfma_f32_16x16x32_bf16(af[G % (PF + 1)], xf[kk][0], SC.first[G] ? z : acc2[nf][0], 0, 0, 0);
      if constexpr (GN < NS) af[GN % (PF + 1)] = afrag(IC(GN));
      acc2[nf][1] = __builtin_amdgcn_mfma_f32_16x16x32_bf16(af[G % (PF + 1)], xf[kk][1], SC.first[G] ? z : acc2[nf][1], 0, 0, 0);
    }
    epi(IC(G));
    if constexpr (SC.pst[G] >= 0) part_store(IC(SC.pst[G]));
    if constexpr (SC.pub[G] != 0) {
      wait_vm<SC.pub_wait[G]>();
      if (lane_now() == 0) set_flag((unsigned*)hfl + ftl, (unsigned)SC.pub[G]);
    }
    sched_fence();
  });

  {
    part_store(IC(8));
    const int ln = lane_now(), gg = ln >> 4, mm = ln & 15;
    wait_vm<0>();
    if (ln == 0 && wave != xcd) set_flag(flags + FL_P + (tile * 8 + wave) * 16 + xcd, 1u);
    lds_barrier();
    u32x4 pv[8]; u32x2 pe[8];
    const GAS unsigned* fp = (const GAS unsigned*)(flags + FL_P + (tile * 8 + xcd) * 16) + (ln & 7);
    unsigned pend = 0xffu & ~(1u << xcd);
#pragma unroll
    for (int s = 0; s < 8; ++s) {
      pv[s] = *(const u32x4*)(smem + OWN + wave * 1024 + ln * 16);
      pe[s] = *(const u32x2*)(smem + OWN + 8 * 1024 + ln * 16 + (wave & 1) * 8);
    }
#pragma nounroll
    for (unsigned spin = 0; spin < SPIN_CAP && pend; ++spin) {
      const unsigned ready = (unsigned)__builtin_amdgcn_readfirstlane((unsigned)__ballot(__hip_atomic_load(fp, RLX_AGENT) != 0u)) & pend;
      mem_fence();
#pragma unroll
      for (int s = 0; s < 8; ++s) {
        if ((ready >> s) & 1u) {
          pv[s] = ld16_sc1(prs, ln * 16, (((xcd * 8 + s) * 32 + tile) * 9 + wave) * 1024);
          if (wave >= 6) pe[s] = ld8_sc1(prs, ln * 16 + (wave & 1) * 8, (((xcd * 8 + s) * 32 + tile) * 9 + 8) * 1024);
        }
      }
      pend &= ~ready;
      if (!ready) __builtin_amdgcn_s_sleep(1);
    }
    float y[3][4] = {};
#pragma unroll
    for (int s = 0; s < 8; ++s) {
#pragma unroll
      for (int t = 0; t < 2; ++t) {
        y[t][0] += bflo(pv[s][2 * t]); y[t][1] += bfhi(pv[s][2 * t]); y[t][2] += bflo(pv[s][2 * t + 1]); y[t][3] += bfhi(pv[s][2 * t + 1]);
      }
      if (wave >= 6) { y[2][0] += bflo(pe[s][0]); y[2][1] += bfhi(pe[s][0]); y[2][2] += bflo(pe[s][1]); y[2][3] += bfhi(pe[s][1]); }
    }
#pragma unroll
    for (int t = 0; t < 3; ++t) {
      if (t == 2 && wave < 6) continue;
      const int nf = t < 2 ? wave : 8, mi = t < 2 ? t : (wave & 1);
      const u32x2 x1 = *(const u32x2*)(smem + p2::X1S + ((nf * 2 + mi) * 64 + ln) * 8);
      const f32x4 gv = *(const f32x4*)(smem + p2::MV + (432 + nf * 16 + 4 * gg) * 4);
      u32x2 o = {resid2(x1[0], gv[0], gv[1], y[t][0], y[t][1]), resid2(x1[1], gv[2], gv[3], y[t][2], y[t][3])};
      *(u32x2*)(out + (size_t)((mt * 8 + xcd) * 32 + mi * 16 + mm) * D_ + ftl * 144 + nf * 16 + 4 * gg) = o;
    }
  }
}

extern "C" __global__ void k_tail() {}

extern "C" void kernel_launch(void* const* d_in, const int* in_sizes, int n_in, void* d_out, int out_size, void* d_ws,
                              size_t ws_size, hipStream_t stream) {
  const u16* x = (const u16*)d_in[0];
  const u16* wqkv = (const u16*)d_in[1];
  const u16* wo = (const u16*)d_in[2];
  const u16* w1 = (const u16*)d_in[4];
  const u16* w3 = (const u16*)d_in[5];
  const u16* w2 = (const u16*)d_in[6];
  const u16* c = (const u16*)d_in[7];
  const u16* wmod = (const u16*)d_in[8];
  (void)hipFuncSetAttribute((const void*)k_main, hipFuncAttributeMaxDynamicSharedMemorySize, LDS_BYTES);
  k_pre<<<k0::NB, 512, 0, stream>>>(x, wqkv, wo, c, wmod, (char*)d_ws);
  k_main<<<256, 512, LDS_BYTES, stream>>>(x, w1, w3, w2, (char*)d_ws, (u16*)d_out);
}
```
